```python
import torch

def _softmax(x, axis=-1):
    return torch.softmax(x, dim=axis)
def _gelu(x):
    return torch.nn.functional.gelu(x, approximate="tanh")
def _silu(x):
    return torch.nn.functional.silu(x)
def _sigmoid(x):
    return torch.sigmoid(x)
def _layernorm(x, axis=-1, eps=1e-5):
    m = x.mean(dim=axis, keepdim=True)
    v = x.var(dim=axis, keepdim=True, unbiased=False)
    return (x - m) / torch.sqrt(v + eps)
def _scatter_add(tgt, idx, src, axis=0):
    return tgt.clone().index_add_(axis, idx, src)
def _segment_sum(src, seg, n):
    out = torch.zeros((n,) + src.shape[1:], dtype=src.dtype)
    return out.index_add_(0, seg, src)
def _pad(x, pw):
    flat = tuple(v for pair in reversed(pw) for v in pair)
    return torch.nn.functional.pad(x, flat)

kernel_name = 'standalone__weight_only_int_omniquant__prompt'

def setup_inputs():

    _w = (lambda a: (lambda t: t.to(torch.bfloat16) if t.is_floating_point() else t)(torch.as_tensor(a)))
    B, S, D, F = 1, 2048, 576, 4608
    bits = 8
    G = -1 if -1 > 0 else D
    D = (D // G) * G
    ng = D // G
    qmax = (1 << bits) - 1
    qw = torch.randint(0, qmax + 1, (D, F)).to(torch.int32)
    scales = (torch.abs((lambda *s: torch.randn(*s, dtype=torch.bfloat16) * 0.05)(ng, F)) * 0.05 + 0.01).to(float)
    zeros = torch.randint(0, qmax + 1, (ng, F)).to(float)
    act_scale = (torch.abs((lambda *s: torch.randn(*s, dtype=torch.bfloat16) * 0.05)(D)) * 0.5 + 0.5).to(float)
    return {
        "x": _w((lambda *s: torch.randn(*s, dtype=torch.bfloat16) * 0.05)(B, S, D)),
        "qw": _w(qw),
        "scales": _w(scales),
        "zeros": _w(zeros),
        "act_scale": _w(act_scale),
    }

def reference(x, qw, scales, zeros, act_scale):
    B, S, D = x.shape
    ng, F = scales.shape
    G = D // ng
    s = (lambda x, n, axis: torch.repeat_interleave(x, n, dim=axis))(scales, G, axis=0)
    z = (lambda x, n, axis: torch.repeat_interleave(x, n, dim=axis))(zeros, G, axis=0)
    w = ((lambda x, dt: x.to(dt))(qw, x.dtype) - z) * s
    xs = x * (lambda x, dt: x.to(dt))(act_scale, x.dtype)
    return torch.einsum("bsd,df->bsf", xs, w)
```

```cpp
#include <hip/hip_runtime.h>
#include <hip/hip_bf16.h>
#include <stdint.h>

typedef __attribute__((ext_vector_type(8))) short bf16x8;
typedef __attribute__((ext_vector_type(4))) float f32x4;
typedef __attribute__((ext_vector_type(4))) int i32x4;
typedef __attribute__((ext_vector_type(2))) float f32x2;
typedef __attribute__((ext_vector_type(2))) __bf16 bf16x2;

constexpr int M = 2048, K = 576, N = 4608;
constexpr int BM = 128, BN = 144, BK = 32;

__device__ __forceinline__ float bf2f(unsigned short h) { return __uint_as_float(((unsigned)h) << 16); }
__device__ __forceinline__ unsigned pk(float a, float b) { f32x2 v = {a, b}; bf16x2 r = __builtin_convertvector(v, bf16x2); return __builtin_bit_cast(unsigned, r); }

__global__ __launch_bounds__(256) void k_base(const unsigned short* __restrict__ x, const int* __restrict__ qw,
    const unsigned short* __restrict__ sc, const unsigned short* __restrict__ zr, const unsigned short* __restrict__ as,
    unsigned short* __restrict__ out) {
  __shared__ __attribute__((aligned(16))) unsigned short xs[BM][BK + 8];
  __shared__ __attribute__((aligned(16))) unsigned short ws[BN][BK + 8];
  const int tid = threadIdx.x, lane = tid & 63, w = tid >> 6;
  const int bm = blockIdx.x % (M / BM), bn = blockIdx.x / (M / BM);
  const int m0 = bm * BM, n0 = bn * BN;
  f32x4 acc[2][9];
  for (int i = 0; i < 2; ++i) for (int j = 0; j < 9; ++j) acc[i][j] = (f32x4){0, 0, 0, 0};
  for (int k0 = 0; k0 < K; k0 += BK) {
    for (int i = tid; i < BM * BK / 8; i += 256) {
      int r = i / (BK / 8), c = i % (BK / 8);
      *(bf16x8*)&xs[r][c * 8] = *(const bf16x8*)&x[(size_t)(m0 + r) * K + k0 + c * 8];
    }
    for (int i = tid; i < BK * BN / 4; i += 256) {
      int r = i / (BN / 4), c = i % (BN / 4);
      i32x4 q = *(const i32x4*)&qw[(size_t)(k0 + r) * N + n0 + c * 4];
      float a = bf2f(as[k0 + r]);
      for (int e = 0; e < 4; ++e) {
        int n = n0 + c * 4 + e;
        float v = ((float)q[e] - bf2f(zr[n])) * bf2f(sc[n]) * a;
        ws[c * 4 + e][r] = (unsigned short)(pk(v, 0.f) & 0xffff);
      }
    }
    __syncthreads();
    bf16x8 xf[2];
    for (int i = 0; i < 2; ++i) xf[i] = *(const bf16x8*)&xs[w * 32 + i * 16 + (lane & 15)][(lane >> 4) * 8];
    for (int j = 0; j < 9; ++j) {
      bf16x8 wf = *(const bf16x8*)&ws[j * 16 + (lane & 15)][(lane >> 4) * 8];
      for (int i = 0; i < 2; ++i) acc[i][j] = __builtin_amdgcn_mfma_f32_16x16x32_bf16(wf, xf[i], acc[i][j], 0, 0, 0);
    }
    __syncthreads();
  }
  for (int i = 0; i < 2; ++i) for (int j = 0; j < 9; ++j) {
    int m = m0 + w * 32 + i * 16 + (lane & 15);
    int n = n0 + j * 16 + (lane >> 4) * 4;
    uint2 o; o.x = pk(acc[i][j][0], acc[i][j][1]); o.y = pk(acc[i][j][2], acc[i][j][3]);
    *(uint2*)&out[(size_t)m * N + n] = o;
  }
}

extern "C" void kernel_launch(void* const* d_in, const int* in_sizes, int n_in,
                              void* d_out, int out_size, void* d_ws, size_t ws_size,
                              hipStream_t stream) {
  k_base<<<(M / BM) * (N / BN), 256, 0, stream>>>((const unsigned short*)d_in[0], (const int*)d_in[1],
      (const unsigned short*)d_in[2], (const unsigned short*)d_in[3], (const unsigned short*)d_in[4], (unsigned short*)d_out);
}
```

```cpp
#include <hip/hip_runtime.h>
#include <hip/hip_bf16.h>
#include <stdint.h>

typedef __attribute__((ext_vector_type(8))) short bf16x8;
typedef __attribute__((ext_vector_type(4))) float f32x4;
typedef __attribute__((ext_vector_type(4))) int i32x4;
typedef __attribute__((ext_vector_type(2))) float f32x2;
typedef __attribute__((ext_vector_type(2))) __bf16 bf16x2;

constexpr int M = 2048, K = 576, N = 4608;
constexpr int BM = 128, BN = 144, BK = 32;

__device__ __forceinline__ float bf2f(unsigned short h) { return __uint_as_float(((unsigned)h) << 16); }
__device__ __forceinline__ unsigned pk(float a, float b) { f32x2 v = {a, b}; bf16x2 r = __builtin_convertvector(v, bf16x2); return __builtin_bit_cast(unsigned, r); }

__global__ __launch_bounds__(256) void k_base(const unsigned short* __restrict__ x, const int* __restrict__ qw,
    const unsigned short* __restrict__ sc, const unsigned short* __restrict__ zr, const unsigned short* __restrict__ as,
    unsigned short* __restrict__ out) {
  __shared__ __attribute__((aligned(16))) unsigned short xs[BM][BK + 8];
  __shared__ __attribute__((aligned(16))) unsigned short ws[BN][BK + 8];
  const int tid = threadIdx.x, lane = tid & 63, w = tid >> 6;
  const int bm = blockIdx.x % (M / BM), bn = blockIdx.x / (M / BM);
  const int m0 = bm * BM, n0 = bn * BN;
  f32x4 acc[2][9];
  for (int i = 0; i < 2; ++i) for (int j = 0; j < 9; ++j) acc[i][j] = (f32x4){0, 0, 0, 0};
  for (int k0 = 0; k0 < K; k0 += BK) {
    for (int i = tid; i < BM * BK / 8; i += 256) {
      int r = i / (BK / 8), c = i % (BK / 8);
      *(bf16x8*)&xs[r][c * 8] = *(const bf16x8*)&x[(size_t)(m0 + r) * K + k0 + c * 8];
    }
    for (int i = tid; i < BK * BN / 4; i += 256) {
      int r = i / (BN / 4), c = i % (BN / 4);
      i32x4 q = *(const i32x4*)&qw[(size_t)(k0 + r) * N + n0 + c * 4];
      float a = bf2f(as[k0 + r]);
      for (int e = 0; e < 4; ++e) {
        int n = n0 + c * 4 + e;
        float v = ((float)q[e] - bf2f(zr[n])) * bf2f(sc[n]) * a;
        ws[c * 4 + e][r] = (unsigned short)(pk(v, 0.f) & 0xffff);
      }
    }
    __syncthreads();
    bf16x8 xf[2];
    for (int i = 0; i < 2; ++i) xf[i] = *(const bf16x8*)&xs[w * 32 + i * 16 + (lane & 15)][(lane >> 4) * 8];
    for (int j = 0; j < 9; ++j) {
      bf16x8 wf = *(const bf16x8*)&ws[j * 16 + (lane & 15)][(lane >> 4) * 8];
      for (int i = 0; i < 2; ++i) acc[i][j] = __builtin_amdgcn_mfma_f32_16x16x32_bf16(wf, xf[i], acc[i][j], 0, 0, 0);
    }
    __syncthreads();
  }
  for (int i = 0; i < 2; ++i) for (int j = 0; j < 9; ++j) {
    int m = m0 + w * 32 + i * 16 + (lane & 15);
    int n = n0 + j * 16 + (lane >> 4) * 4;
    uint2 o; o.x = pk(acc[i][j][0], acc[i][j][1]); o.y = pk(acc[i][j][2], acc[i][j][3]);
    *(uint2*)&out[(size_t)m * N + n] = o;
  }
}


typedef __attribute__((ext_vector_type(4))) int pi32x4;
__global__ __launch_bounds__(256) void k_probe(const char* __restrict__ p, int* sink) {
  const char* b = p + (size_t)blockIdx.x * 40960;
  pi32x4 q[10];
#pragma unroll
  for (int j = 0; j < 10; ++j) { unsigned off = j * 4096 + threadIdx.x * 16; asm volatile("global_load_dwordx4 %0, %1, %2 " : "=v"(q[j]) : "v"(off), "s"(b) : "memory"); }
  asm volatile("s_waitcnt vmcnt(0)" ::: "memory");
  int a = 0;
#pragma unroll
  for (int j = 0; j < 10; ++j) { asm volatile("" : "+v"(q[j])); a ^= q[j][0] ^ q[j][1] ^ (q[j][2] * 3) ^ (q[j][3] * 5); }
  if (a == 0x5a17c3e9) sink[threadIdx.x] = a;
}

extern "C" void kernel_launch(void* const* d_in, const int* in_sizes, int n_in,
                              void* d_out, int out_size, void* d_ws, size_t ws_size,
                              hipStream_t stream) {
  k_probe<<<256, 256, 0, stream>>>((const char*)d_in[1], (int*)d_ws);
  k_base<<<(M / BM) * (N / BN), 256, 0, stream>>>((const unsigned short*)d_in[0], (const int*)d_in[1],
      (const unsigned short*)d_in[2], (const unsigned short*)d_in[3], (const unsigned short*)d_in[4], (unsigned short*)d_out);
}
```

```cpp
#include <hip/hip_runtime.h>
#include <hip/hip_bf16.h>
#include <stdint.h>

typedef __attribute__((ext_vector_type(8))) short bf16x8;
typedef __attribute__((ext_vector_type(4))) float f32x4;
typedef __attribute__((ext_vector_type(4))) int i32x4;
typedef __attribute__((ext_vector_type(2))) float f32x2;
typedef __attribute__((ext_vector_type(2))) __bf16 bf16x2;

constexpr int M = 2048, K = 576, N = 4608;
constexpr int BM = 128, BN = 144, BK = 32;

__device__ __forceinline__ float bf2f(unsigned short h) { return __uint_as_float(((unsigned)h) << 16); }
__device__ __forceinline__ unsigned pk(float a, float b) { f32x2 v = {a, b}; bf16x2 r = __builtin_convertvector(v, bf16x2); return __builtin_bit_cast(unsigned, r); }

__global__ __launch_bounds__(256) void k_base(const unsigned short* __restrict__ x, const int* __restrict__ qw,
    const unsigned short* __restrict__ sc, const unsigned short* __restrict__ zr, const unsigned short* __restrict__ as,
    unsigned short* out, const char* probe, int* sink) {
  __shared__ __attribute__((aligned(16))) unsigned short xs[BM][BK + 8];
  __shared__ __attribute__((aligned(16))) unsigned short ws[BN][BK + 8];
  const int tid = threadIdx.x, lane = tid & 63, w = tid >> 6;
  const int bm = blockIdx.x % (M / BM), bn = blockIdx.x / (M / BM);
  const int m0 = bm * BM, n0 = bn * BN;

  {
    typedef __attribute__((ext_vector_type(4))) int pi32x4;
    const char* b = probe + (size_t)blockIdx.x * 81920;
    pi32x4 q[1];
#pragma unroll
    for (int j = 0; j < 0; ++j) { unsigned off = j * 4096 + threadIdx.x * 16; asm volatile("global_load_dwordx4 %0, %1, %2 " : "=v"(q[j]) : "v"(off), "s"(b) : "memory"); }
    asm volatile("s_waitcnt vmcnt(0)" ::: "memory");
    int a = 0;
#pragma unroll
    for (int j = 0; j < 0; ++j) { asm volatile("" : "+v"(q[j])); a ^= q[j][0] ^ q[j][1] ^ (q[j][2] * 3) ^ (q[j][3] * 5); }
    if (a == 0x5a17c3e9) sink[threadIdx.x] = a;
  }
  f32x4 acc[2][9];
  for (int i = 0; i < 2; ++i) for (int j = 0; j < 9; ++j) acc[i][j] = (f32x4){0, 0, 0, 0};
  for (int k0 = 0; k0 < K; k0 += BK) {
    for (int i = tid; i < BM * BK / 8; i += 256) {
      int r = i / (BK / 8), c = i % (BK / 8);
      *(bf16x8*)&xs[r][c * 8] = *(const bf16x8*)&x[(size_t)(m0 + r) * K + k0 + c * 8];
    }
    for (int i = tid; i < BK * BN / 4; i += 256) {
      int r = i / (BN / 4), c = i % (BN / 4);
      i32x4 q = *(const i32x4*)&qw[(size_t)(k0 + r) * N + n0 + c * 4];
      float a = bf2f(as[k0 + r]);
      for (int e = 0; e < 4; ++e) {
        int n = n0 + c * 4 + e;
        float v = ((float)q[e] - bf2f(zr[n])) * bf2f(sc[n]) * a;
        ws[c * 4 + e][r] = (unsigned short)(pk(v, 0.f) & 0xffff);
      }
    }
    __syncthreads();
    bf16x8 xf[2];
    for (int i = 0; i < 2; ++i) xf[i] = *(const bf16x8*)&xs[w * 32 + i * 16 + (lane & 15)][(lane >> 4) * 8];
    for (int j = 0; j < 9; ++j) {
      bf16x8 wf = *(const bf16x8*)&ws[j * 16 + (lane & 15)][(lane >> 4) * 8];
      for (int i = 0; i < 2; ++i) acc[i][j] = __builtin_amdgcn_mfma_f32_16x16x32_bf16(wf, xf[i], acc[i][j], 0, 0, 0);
    }
    __syncthreads();
  }
  for (int i = 0; i < 2; ++i) for (int j = 0; j < 9; ++j) {
    int m = m0 + w * 32 + i * 16 + (lane & 15);
    int n = n0 + j * 16 + (lane >> 4) * 4;
    uint2 o; o.x = pk(acc[i][j][0], acc[i][j][1]); o.y = pk(acc[i][j][2], acc[i][j][3]);
    *(uint2*)&out[(size_t)m * N + n] = o;
  }
}

extern "C" void kernel_launch(void* const* d_in, const int* in_sizes, int n_in,
                              void* d_out, int out_size, void* d_ws, size_t ws_size,
                              hipStream_t stream) {
  k_base<<<(M / BM) * (N / BN), 256, 0, stream>>>((const unsigned short*)d_in[0], (const int*)d_in[1],
      (const unsigned short*)d_in[2], (const unsigned short*)d_in[3], (const unsigned short*)d_in[4], (unsigned short*)d_out, (const char*)d_ws + 4096, (int*)d_ws);
}
```

```cpp
#include <hip/hip_runtime.h>
#include <hip/hip_bf16.h>
#include <stdint.h>

typedef __attribute__((ext_vector_type(8))) short bf16x8;
typedef __attribute__((ext_vector_type(4))) float f32x4;
typedef __attribute__((ext_vector_type(4))) int i32x4;
typedef __attribute__((ext_vector_type(2))) float f32x2;
typedef __attribute__((ext_vector_type(2))) __bf16 bf16x2;

constexpr int M = 2048, K = 576, N = 4608;
constexpr int BM = 128, BN = 144, BK = 32;

__device__ __forceinline__ float bf2f(unsigned short h) { return __uint_as_float(((unsigned)h) << 16); }
__device__ __forceinline__ unsigned pk(float a, float b) { f32x2 v = {a, b}; bf16x2 r = __builtin_convertvector(v, bf16x2); return __builtin_bit_cast(unsigned, r); }

__global__ __launch_bounds__(256) void k_base(const unsigned short* __restrict__ x, const int* __restrict__ qw,
    const unsigned short* __restrict__ sc, const unsigned short* __restrict__ zr, const unsigned short* __restrict__ as,
    unsigned short* __restrict__ out) {
  __shared__ __attribute__((aligned(16))) unsigned short xs[BM][BK + 8];
  __shared__ __attribute__((aligned(16))) unsigned short ws[BN][BK + 8];
  unsigned long long r0 = __builtin_amdgcn_s_memrealtime();
  const int tid = threadIdx.x, lane = tid & 63, w = tid >> 6;
  const int bm = blockIdx.x % (M / BM), bn = blockIdx.x / (M / BM);
  const int m0 = bm * BM, n0 = bn * BN;
  f32x4 acc[2][9];
  for (int i = 0; i < 2; ++i) for (int j = 0; j < 9; ++j) acc[i][j] = (f32x4){0, 0, 0, 0};
  for (int k0 = 0; k0 < K; k0 += BK) {
    for (int i = tid; i < BM * BK / 8; i += 256) {
      int r = i / (BK / 8), c = i % (BK / 8);
      *(bf16x8*)&xs[r][c * 8] = *(const bf16x8*)&x[(size_t)(m0 + r) * K + k0 + c * 8];
    }
    for (int i = tid; i < BK * BN / 4; i += 256) {
      int r = i / (BN / 4), c = i % (BN / 4);
      i32x4 q = *(const i32x4*)&qw[(size_t)(k0 + r) * N + n0 + c * 4];
      float a = bf2f(as[k0 + r]);
      for (int e = 0; e < 4; ++e) {
        int n = n0 + c * 4 + e;
        float v = ((float)q[e] - bf2f(zr[n])) * bf2f(sc[n]) * a;
        ws[c * 4 + e][r] = (unsigned short)(pk(v, 0.f) & 0xffff);
      }
    }
    __syncthreads();
    bf16x8 xf[2];
    for (int i = 0; i < 2; ++i) xf[i] = *(const bf16x8*)&xs[w * 32 + i * 16 + (lane & 15)][(lane >> 4) * 8];
    for (int j = 0; j < 9; ++j) {
      bf16x8 wf = *(const bf16x8*)&ws[j * 16 + (lane & 15)][(lane >> 4) * 8];
      for (int i = 0; i < 2; ++i) acc[i][j] = __builtin_amdgcn_mfma_f32_16x16x32_bf16(wf, xf[i], acc[i][j], 0, 0, 0);
    }
    __syncthreads();
  }
  for (int i = 0; i < 2; ++i) for (int j = 0; j < 9; ++j) {
    int m = m0 + w * 32 + i * 16 + (lane & 15);
    int n = n0 + j * 16 + (lane >> 4) * 4;
    uint2 o; o.x = pk(acc[i][j][0], acc[i][j][1]); o.y = pk(acc[i][j][2], acc[i][j][3]);
    *(uint2*)&out[(size_t)m * N + n] = o;
  }
  asm volatile("s_waitcnt vmcnt(0)" ::: "memory");
  unsigned long long r1 = __builtin_amdgcn_s_memrealtime();
  unsigned d = (unsigned)(r1 - r0) * 0u; if (d > 20000u) d = 20000u;
  while ((unsigned)(__builtin_amdgcn_s_memrealtime() - r1) < d) __builtin_amdgcn_s_sleep(1);
}

extern "C" void kernel_launch(void* const* d_in, const int* in_sizes, int n_in,
                              void* d_out, int out_size, void* d_ws, size_t ws_size,
                              hipStream_t stream) {
  k_base<<<(M / BM) * (N / BN), 256, 0, stream>>>((const unsigned short*)d_in[0], (const int*)d_in[1],
      (const unsigned short*)d_in[2], (const unsigned short*)d_in[3], (const unsigned short*)d_in[4], (unsigned short*)d_out);
}
```

```cpp
#include <hip/hip_runtime.h>
#include <stdint.h>

__global__ __launch_bounds__(512, 2) void standalone__weight_only_int_omniquant__prompt_kernel(
    const char* x, const char* qw, const char* sc, const char* zr, const char* act, char* out) {
  __shared__ __attribute__((aligned(1024))) char smem[143616];
  const unsigned bid = blockIdx.x;
  const unsigned wv = __builtin_amdgcn_readfirstlane(threadIdx.x >> 6);
  const unsigned ldsb = (unsigned)(size_t)(__attribute__((address_space(3))) char*)smem;
  const void* karg = (const void*)__builtin_amdgcn_kernarg_segment_ptr();
  asm volatile(
    "L_start%=:\n\t"
    "s_load_dwordx2 s[24:25], %[karg], 0x0\n\t"
    "s_load_dwordx2 s[20:21], %[karg], 0x8\n\t"
    "s_load_dwordx2 s[28:29], %[karg], 0x10\n\t"
    "s_load_dwordx2 s[8:9], %[karg], 0x18\n\t"
    "s_load_dwordx2 s[36:37], %[karg], 0x20\n\t"
    "s_load_dwordx2 s[40:41], %[karg], 0x28\n\t"
    "v_mbcnt_lo_u32_b32 v0, -1, 0\n\t"
    "v_mbcnt_hi_u32_b32 v0, -1, v0\n\t"
    "s_cmp_lg_u32 %[wv], 4\n\t"
    "s_cbranch_scc1 L_nopf%=\n\t"
    "s_getpc_b64 s[6:7]\n\t"
    "v_lshlrev_b32 v1, 7, v0\n\t"
    "v_min_u32 v2, L_end%=-L_start%=-128, v1\n\t"
    "global_load_dword v255, v2, s[6:7]\n\t"
    "v_add_u32 v1, 8192, v1\n\t"
    "v_min_u32 v2, L_end%=-L_start%=-128, v1\n\t"
    "global_load_dword v255, v2, s[6:7]\n\t"
    "L_nopf%=:\n\t"
    "s_mov_b32 s26, 2359296\n\t"
    "s_mov_b32 s27, 0x20000\n\t"
    "s_mov_b32 s22, 10616832\n\t"
    "s_mov_b32 s23, 0x20000\n\t"
    "s_mov_b32 s30, 9216\n\t"
    "s_mov_b32 s31, 0x20000\n\t"
    "s_mov_b32 s10, 9216\n\t"
    "s_mov_b32 s11, 0x20000\n\t"
    "s_mov_b32 s38, 1152\n\t"
    "s_mov_b32 s39, 0x20000\n\t"
    "s_mov_b32 s42, 18874368\n\t"
    "s_mov_b32 s43, 0x20000\n\t"
    "s_and_b32 s70, %[bid], 7\n\t"
    "s_lshr_b32 s71, %[bid], 3\n\t"
    "s_and_b32 s66, s71, 7\n\t"
    "s_lshr_b32 s72, s71, 3\n\t"
    "s_lshl_b32 s68, s66, 8\n\t"
    "s_and_b32 s64, %[wv], 3\n\t"
    "s_mul_i32 s69, s70, 576\n\t"
    "s_lshl_b32 s71, s72, 4\n\t"
    "s_add_u32 s71, s71, s69\n\t"
    "s_add_u32 s71, s71, 512\n\t"
    "s_lshl_b32 s70, s72, 7\n\t"
    "s_add_u32 s70, s70, s69\n\t"
    "s_cmp_lt_u32 %[wv], 4\n\t"
    "s_cbranch_scc0 L_consumer%=\n\t"
    "s_setprio 1\n\t"
    "s_and_b32 s72, s64, 1\n\t"
    "s_lshl_b32 s72, s72, 6\n\t"
    "s_add_u32 s70, s70, s72\n\t"
    "v_lshrrev_b32 v1, 3, v0\n\t"
    "v_and_b32 v2, 7, v0\n\t"
    "v_lshrrev_b32 v3, 2, v0\n\t"
    "v_and_b32 v114, 3, v0\n\t"
    "s_lshl_b32 s73, s70, 1\n\t"
    "v_lshl_add_u32 v154, v2, 3, s73\n\t"
    "s_lshl_b32 s73, s71, 1\n\t"
    "v_lshl_add_u32 v155, v114, 3, s73\n\t"
    "s_lshr_b32 s72, s64, 1\n\t"
    "s_lshl_b32 s72, s72, 5\n\t"
    "v_lshl_add_u32 v112, v1, 2, s72\n\t"
    "v_lshlrev_b32 v156, 1, v112\n\t"
    "v_mul_u32_u24 v112, 18432, v112\n\t"
    "s_lshl_b32 s73, s70, 2\n\t"
    "v_lshl_add_u32 v113, v2, 4, s73\n\t"
    "v_add_u32 v136, v112, v113\n\t"
    "v_add_u32 v137, 18432, v136\n\t"
    "v_add_u32 v138, 36864, v136\n\t"
    "v_add_u32 v139, 55296, v136\n\t"
    "s_lshl_b32 s72, s64, 4\n\t"
    "v_add_u32 v112, s72, v3\n\t"
    "v_lshlrev_b32 v157, 1, v112\n\t"
    "v_mul_u32_u24 v112, 18432, v112\n\t"
    "s_lshl_b32 s73, s71, 2\n\t"
    "v_lshl_add_u32 v113, v114, 4, s73\n\t"
    "v_add_u32 v140, v112, v113\n\t"
    "v_lshrrev_b32 v112, 4, v0\n\t"
    "v_xor_b32 v112, v112, v2\n\t"
    "v_lshlrev_b32 v112, 4, v112\n\t"
    "v_mul_u32_u24 v113, 1152, v1\n\t"
    "v_add_u32 v112, v112, v113\n\t"
    "v_xor_b32 v113, 64, v112\n\t"
    "s_lshl_b32 s72, s64, 6\n\t"
    "s_add_u32 s72, s72, s68\n\t"
    "s_mul_i32 s72, s72, 1152\n\t"
    "s_add_u32 s73, s72, 0\n\t"
    "v_add_u32 v142, s73, v112\n\t"
    "s_add_u32 s73, s72, 8192\n\t"
    "v_add_u32 v143, s73, v113\n\t"
    "s_add_u32 s73, s72, 16384\n\t"
    "v_add_u32 v144, s73, v112\n\t"
    "s_add_u32 s73, s72, 24576\n\t"
    "v_add_u32 v145, s73, v113\n\t"
    "s_add_u32 s73, s72, 36864\n\t"
    "v_add_u32 v146, s73, v112\n\t"
    "s_add_u32 s73, s72, 45056\n\t"
    "v_add_u32 v147, s73, v113\n\t"
    "s_add_u32 s73, s72, 53248\n\t"
    "v_add_u32 v148, s73, v112\n\t"
    "s_add_u32 s73, s72, 61440\n\t"
    "v_add_u32 v149, s73, v113\n\t"
    "s_lshl_b32 s74, s64, 13\n\t"
    "s_lshl_b32 s19, s64, 11\n\t"
    "s_add_u32 s19, s19, 135424\n\t"
    "v_lshlrev_b32 v172, 4, v0\n\t"
    "v_add_u32 v173, 1024, v172\n\t"
    "v_add_u32 v156, s19, v156\n\t"
    "v_add_u32 v157, s19, v157\n\t"
    "s_and_b32 s83, %[bid], 7\n\t"
    "s_mul_i32 s83, s83, 3\n\t"
    "s_add_u32 s83, s83, s66\n\t"
    "s_mul_i32 s72, s83, 57\n\t"
    "s_lshr_b32 s72, s72, 9\n\t"
    "s_mul_i32 s72, s72, 9\n\t"
    "s_sub_u32 s83, s83, s72\n\t"
    "s_mul_i32 s44, s83, 1179648\n\t"
    "s_lshl_b32 s53, s83, 7\n\t"
    "s_mov_b32 m0, s19\n\t"
    "s_waitcnt lgkmcnt(0)\n\t"
    "s_and_b32 s37, s37, 0xffff\n\t"
    "s_and_b32 s29, s29, 0xffff\n\t"
    "s_and_b32 s9, s9, 0xffff\n\t"
    "s_and_b32 s21, s21, 0xffff\n\t"
    "s_and_b32 s25, s25, 0xffff\n\t"
    "buffer_load_dwordx4 v172, s[36:39], 0 offen lds\n\t"
    "s_add_u32 m0, s19, 1024\n\t"
    "s_nop 0\n\t"
    "buffer_load_dwordx4 v173, s[36:39], 0 offen lds\n\t"
    "s_mov_b32 m0, s74\n\t"
    "buffer_load_dwordx2 v[160:161], v154, s[28:31], 0 offen offset:0\n\t"
    "buffer_load_dwordx2 v[166:167], v154, s[8:11], 0 offen offset:0\n\t"
    "buffer_load_dwordx2 v[162:163], v154, s[28:31], 0 offen offset:64\n\t"
    "buffer_load_dwordx2 v[168:169], v154, s[8:11], 0 offen offset:64\n\t"
    "buffer_load_dwordx2 v[164:165], v155, s[28:31], 0 offen\n\t"
    "buffer_load_dwordx2 v[170:171], v155, s[8:11], 0 offen\n\t"
    "buffer_load_dwordx4 v[4:7], v136, s[20:23], s44 offen offset:0\n\t"
    "buffer_load_dwordx4 v142, s[24:27], s53 offen offset:0  lds\n\t"
    "buffer_load_dwordx4 v[8:11], v137, s[20:23], s44 offen offset:0\n\t"
    "buffer_load_dwordx4 v143, s[24:27], s53 offen offset:1024  lds\n\t"
    "buffer_load_dwordx4 v[12:15], v138, s[20:23], s44 offen offset:0\n\t"
    "buffer_load_dwordx4 v144, s[24:27], s53 offen offset:2048  lds\n\t"
    "buffer_load_dwordx4 v[16:19], v139, s[20:23], s44 offen offset:0\n\t"
    "buffer_load_dwordx4 v145, s[24:27], s53 offen offset:3072  lds\n\t"
    "buffer_load_dwordx4 v[20:23], v136, s[20:23], s44 offen offset:128\n\t"
    "v_mov_b32 v64, 0\n\t"
    "v_mov_b32 v65, 0\n\t"
    "v_mov_b32 v66, 0\n\t"
    "v_mov_b32 v67, 0\n\t"
    "v_mov_b32 v68, 0\n\t"
    "v_mov_b32 v69, 0\n\t"
    "v_mov_b32 v70, 0\n\t"
    "v_mov_b32 v71, 0\n\t"
    "v_mov_b32 v72, 0\n\t"
    "v_mov_b32 v73, 0\n\t"
    "v_mov_b32 v74, 0\n\t"
    "v_mov_b32 v75, 0\n\t"
    "v_mov_b32 v76, 0\n\t"
    "v_mov_b32 v77, 0\n\t"
    "v_mov_b32 v78, 0\n\t"
    "v_mov_b32 v79, 0\n\t"
    "v_mov_b32 v80, 0\n\t"
    "v_mov_b32 v81, 0\n\t"
    "v_mov_b32 v82, 0\n\t"
    "v_mov_b32 v83, 0\n\t"
    "v_mov_b32 v84, 0\n\t"
    "v_mov_b32 v85, 0\n\t"
    "v_mov_b32 v86, 0\n\t"
    "v_mov_b32 v87, 0\n\t"
    "v_mov_b32 v88, 0\n\t"
    "v_mov_b32 v89, 0\n\t"
    "v_mov_b32 v90, 0\n\t"
    "v_mov_b32 v91, 0\n\t"
    "v_mov_b32 v92, 0\n\t"
    "v_mov_b32 v93, 0\n\t"
    "v_mov_b32 v94, 0\n\t"
    "v_mov_b32 v95, 0\n\t"
    "v_mov_b32 v96, 0\n\t"
    "v_mov_b32 v97, 0\n\t"
    "v_mov_b32 v98, 0\n\t"
    "v_mov_b32 v99, 0\n\t"
    "v_mov_b32 v100, 0\n\t"
    "v_mov_b32 v101, 0\n\t"
    "v_mov_b32 v102, 0\n\t"
    "v_mov_b32 v103, 0\n\t"
    "v_mov_b32 v104, 0\n\t"
    "v_mov_b32 v105, 0\n\t"
    "v_mov_b32 v106, 0\n\t"
    "v_mov_b32 v107, 0\n\t"
    "v_mov_b32 v108, 0\n\t"
    "s_add_u32 s83, s83, 1\n\t"
    "s_cmp_eq_u32 s83, 9\n\t"
    "s_cselect_b32 s83, 0, s83\n\t"
    "s_mul_i32 s45, s83, 1179648\n\t"
    "s_lshl_b32 s54, s83, 7\n\t"
    "s_add_u32 s83, s83, 1\n\t"
    "s_cmp_eq_u32 s83, 9\n\t"
    "s_cselect_b32 s83, 0, s83\n\t"
    "s_mul_i32 s46, s83, 1179648\n\t"
    "s_lshl_b32 s55, s83, 7\n\t"
    "s_add_u32 s83, s83, 1\n\t"
    "s_cmp_eq_u32 s83, 9\n\t"
    "s_cselect_b32 s83, 0, s83\n\t"
    "s_mul_i32 s47, s83, 1179648\n\t"
    "s_lshl_b32 s56, s83, 7\n\t"
    "s_add_u32 s83, s83, 1\n\t"
    "s_cmp_eq_u32 s83, 9\n\t"
    "s_cselect_b32 s83, 0, s83\n\t"
    "s_mul_i32 s48, s83, 1179648\n\t"
    "s_lshl_b32 s57, s83, 7\n\t"
    "s_add_u32 s83, s83, 1\n\t"
    "s_cmp_eq_u32 s83, 9\n\t"
    "s_cselect_b32 s83, 0, s83\n\t"
    "s_mul_i32 s49, s83, 1179648\n\t"
    "s_lshl_b32 s58, s83, 7\n\t"
    "s_add_u32 s83, s83, 1\n\t"
    "s_cmp_eq_u32 s83, 9\n\t"
    "s_cselect_b32 s83, 0, s83\n\t"
    "s_mul_i32 s50, s83, 1179648\n\t"
    "s_lshl_b32 s59, s83, 7\n\t"
    "s_add_u32 s83, s83, 1\n\t"
    "s_cmp_eq_u32 s83, 9\n\t"
    "s_cselect_b32 s83, 0, s83\n\t"
    "s_mul_i32 s51, s83, 1179648\n\t"
    "s_lshl_b32 s60, s83, 7\n\t"
    "s_add_u32 s83, s83, 1\n\t"
    "s_cmp_eq_u32 s83, 9\n\t"
    "s_cselect_b32 s83, 0, s83\n\t"
    "s_mul_i32 s52, s83, 1179648\n\t"
    "s_lshl_b32 s61, s83, 7\n\t"
    "v_bfe_u32 v112, v0, 1, 2\n\t"
    "v_lshrrev_b32 v113, 4, v0\n\t"
    "v_xor_b32 v113, v113, v112\n\t"
    "v_lshlrev_b32 v114, 6, v112\n\t"
    "v_bfe_u32 v115, v0, 3, 1\n\t"
    "v_lshl_add_u32 v114, v115, 3, v114\n\t"
    "v_and_b32 v115, 1, v0\n\t"
    "v_mul_u32_u24 v115, 8256, v115\n\t"
    "v_add_u32 v114, v114, v115\n\t"
    "s_and_b32 s71, s64, 1\n\t"
    "s_lshr_b32 s70, s64, 1\n\t"
    "s_lshl_b32 s70, s70, 10\n\t"
    "s_lshl_b32 s72, s71, 9\n\t"
    "s_add_u32 s70, s70, s72\n\t"
    "s_add_u32 s70, s70, 98304\n\t"
    "s_lshl_b32 s72, s71, 1\n\t"
    "s_add_u32 s72, s72, 0\n\t"
    "s_sub_u32 s72, 0, s72\n\t"
    "s_and_b32 s72, s72, 3\n\t"
    "v_xor_b32 v115, s72, v113\n\t"
    "v_lshl_add_u32 v115, v115, 4, v114\n\t"
    "s_add_u32 s73, s70, 0\n\t"
    "v_add_u32 v150, s73, v115\n\t"
    "s_lshl_b32 s72, s71, 1\n\t"
    "s_add_u32 s72, s72, 1\n\t"
    "s_sub_u32 s72, 0, s72\n\t"
    "s_and_b32 s72, s72, 3\n\t"
    "v_xor_b32 v115, s72, v113\n\t"
    "v_lshl_add_u32 v115, v115, 4, v114\n\t"
    "s_add_u32 s73, s70, 256\n\t"
    "v_add_u32 v151, s73, v115\n\t"
    "s_lshr_b32 s70, s64, 1\n\t"
    "s_lshl_b32 s70, s70, 5\n\t"
    "s_lshl_b32 s71, s71, 3\n\t"
    "s_add_u32 s70, s70, s71\n\t"
    "v_bfe_u32 v112, v0, 4, 1\n\t"
    "v_lshlrev_b32 v112, 4, v112\n\t"
    "v_lshrrev_b32 v113, 5, v0\n\t"
    "v_lshl_add_u32 v112, v113, 2, v112\n\t"
    "v_and_b32 v113, 3, v3\n\t"
    "v_add_u32 v112, v112, v113\n\t"
    "v_add_u32 v112, s70, v112\n\t"
    "v_lshlrev_b32 v112, 5, v112\n\t"
    "v_and_b32 v113, 3, v0\n\t"
    "v_lshl_add_u32 v112, v113, 3, v112\n\t"
    "v_add_u32 v152, 114816, v112\n\t"
    "s_waitcnt vmcnt(9)\n\t"
    "v_add_u32 v112, s53, v156\n\t"
    "v_add_u32 v113, s53, v157\n\t"
    "ds_read_u16_d16_hi v64, v112 offset:0\n\t"
    "ds_read_u16_d16_hi v65, v112 offset:2\n\t"
    "ds_read_u16_d16_hi v66, v112 offset:4\n\t"
    "ds_read_u16_d16_hi v67, v112 offset:6\n\t"
    "ds_read_u16_d16_hi v68, v113\n\t"
    "v_add_u32 v112, s54, v156\n\t"
    "v_add_u32 v113, s54, v157\n\t"
    "ds_read_u16_d16_hi v69, v112 offset:0\n\t"
    "ds_read_u16_d16_hi v70, v112 offset:2\n\t"
    "ds_read_u16_d16_hi v71, v112 offset:4\n\t"
    "ds_read_u16_d16_hi v72, v112 offset:6\n\t"
    "ds_read_u16_d16_hi v73, v113\n\t"
    "v_add_u32 v112, s55, v156\n\t"
    "v_add_u32 v113, s55, v157\n\t"
    "ds_read_u16_d16_hi v74, v112 offset:0\n\t"
    "ds_read_u16_d16_hi v75, v112 offset:2\n\t"
    "ds_read_u16_d16_hi v76, v112 offset:4\n\t"
    "ds_read_u16_d16_hi v77, v112 offset:6\n\t"
    "ds_read_u16_d16_hi v78, v113\n\t"
    "v_add_u32 v112, s56, v156\n\t"
    "v_add_u32 v113, s56, v157\n\t"
    "ds_read_u16_d16_hi v79, v112 offset:0\n\t"
    "ds_read_u16_d16_hi v80, v112 offset:2\n\t"
    "ds_read_u16_d16_hi v81, v112 offset:4\n\t"
    "ds_read_u16_d16_hi v82, v112 offset:6\n\t"
    "ds_read_u16_d16_hi v83, v113\n\t"
    "v_add_u32 v112, s57, v156\n\t"
    "v_add_u32 v113, s57, v157\n\t"
    "ds_read_u16_d16_hi v84, v112 offset:0\n\t"
    "ds_read_u16_d16_hi v85, v112 offset:2\n\t"
    "ds_read_u16_d16_hi v86, v112 offset:4\n\t"
    "ds_read_u16_d16_hi v87, v112 offset:6\n\t"
    "ds_read_u16_d16_hi v88, v113\n\t"
    "v_add_u32 v112, s58, v156\n\t"
    "v_add_u32 v113, s58, v157\n\t"
    "ds_read_u16_d16_hi v89, v112 offset:0\n\t"
    "ds_read_u16_d16_hi v90, v112 offset:2\n\t"
    "ds_read_u16_d16_hi v91, v112 offset:4\n\t"
    "ds_read_u16_d16_hi v92, v112 offset:6\n\t"
    "ds_read_u16_d16_hi v93, v113\n\t"
    "v_add_u32 v112, s59, v156\n\t"
    "v_add_u32 v113, s59, v157\n\t"
    "ds_read_u16_d16_hi v94, v112 offset:0\n\t"
    "ds_read_u16_d16_hi v95, v112 offset:2\n\t"
    "ds_read_u16_d16_hi v96, v112 offset:4\n\t"
    "ds_read_u16_d16_hi v97, v112 offset:6\n\t"
    "ds_read_u16_d16_hi v98, v113\n\t"
    "v_add_u32 v112, s60, v156\n\t"
    "v_add_u32 v113, s60, v157\n\t"
    "ds_read_u16_d16_hi v99, v112 offset:0\n\t"
    "ds_read_u16_d16_hi v100, v112 offset:2\n\t"
    "ds_read_u16_d16_hi v101, v112 offset:4\n\t"
    "ds_read_u16_d16_hi v102, v112 offset:6\n\t"
    "ds_read_u16_d16_hi v103, v113\n\t"
    "v_add_u32 v112, s61, v156\n\t"
    "v_add_u32 v113, s61, v157\n\t"
    "ds_read_u16_d16_hi v104, v112 offset:0\n\t"
    "ds_read_u16_d16_hi v105, v112 offset:2\n\t"
    "ds_read_u16_d16_hi v106, v112 offset:4\n\t"
    "ds_read_u16_d16_hi v107, v112 offset:6\n\t"
    "ds_read_u16_d16_hi v108, v113\n\t"
    "s_mov_b32 s82, 0x4b800000\n\t"
    "v_lshlrev_b32 v40, 16, v160\n\t"
    "v_lshlrev_b32 v52, 16, v166\n\t"
    "v_and_b32 v41, 0xffff0000, v160\n\t"
    "v_and_b32 v53, 0xffff0000, v166\n\t"
    "v_lshlrev_b32 v42, 16, v161\n\t"
    "v_lshlrev_b32 v54, 16, v167\n\t"
    "v_and_b32 v43, 0xffff0000, v161\n\t"
    "v_and_b32 v55, 0xffff0000, v167\n\t"
    "v_mul_f32_e64 v52, -v52, v40\n\t"
    "v_mul_f32_e64 v53, -v53, v41\n\t"
    "v_mul_f32_e64 v54, -v54, v42\n\t"
    "v_mul_f32_e64 v55, -v55, v43\n\t"
    "v_mul_f32 v40, s82, v40\n\t"
    "v_mul_f32 v41, s82, v41\n\t"
    "v_mul_f32 v42, s82, v42\n\t"
    "v_mul_f32 v43, s82, v43\n\t"
    "v_lshlrev_b32 v44, 16, v162\n\t"
    "v_lshlrev_b32 v56, 16, v168\n\t"
    "v_and_b32 v45, 0xffff0000, v162\n\t"
    "v_and_b32 v57, 0xffff0000, v168\n\t"
    "v_lshlrev_b32 v46, 16, v163\n\t"
    "v_lshlrev_b32 v58, 16, v169\n\t"
    "v_and_b32 v47, 0xffff0000, v163\n\t"
    "v_and_b32 v59, 0xffff0000, v169\n\t"
    "v_mul_f32_e64 v56, -v56, v44\n\t"
    "v_mul_f32_e64 v57, -v57, v45\n\t"
    "v_mul_f32_e64 v58, -v58, v46\n\t"
    "v_mul_f32_e64 v59, -v59, v47\n\t"
    "v_mul_f32 v44, s82, v44\n\t"
    "v_mul_f32 v45, s82, v45\n\t"
    "v_mul_f32 v46, s82, v46\n\t"
    "v_mul_f32 v47, s82, v47\n\t"
    "v_lshlrev_b32 v48, 16, v164\n\t"
    "v_lshlrev_b32 v60, 16, v170\n\t"
    "v_and_b32 v49, 0xffff0000, v164\n\t"
    "v_and_b32 v61, 0xffff0000, v170\n\t"
    "v_lshlrev_b32 v50, 16, v165\n\t"
    "v_lshlrev_b32 v62, 16, v171\n\t"
    "v_and_b32 v51, 0xffff0000, v165\n\t"
    "v_and_b32 v63, 0xffff0000, v171\n\t"
    "v_mul_f32_e64 v60, -v60, v48\n\t"
    "v_mul_f32_e64 v61, -v61, v49\n\t"
    "v_mul_f32_e64 v62, -v62, v50\n\t"
    "v_mul_f32_e64 v63, -v63, v51\n\t"
    "v_mul_f32 v48, s82, v48\n\t"
    "v_mul_f32 v49, s82, v49\n\t"
    "v_mul_f32 v50, s82, v50\n\t"
    "v_mul_f32 v51, s82, v51\n\t"
    "s_add_u32 m0, s74, 4096\n\t"
    "s_nop 0\n\t"
    "buffer_load_dwordx4 v146, s[24:27], s53 offen offset:0  lds\n\t"
    "buffer_load_dwordx4 v[24:27], v137, s[20:23], s44 offen offset:128\n\t"
    "buffer_load_dwordx4 v147, s[24:27], s53 offen offset:1024  lds\n\t"
    "buffer_load_dwordx4 v[28:31], v138, s[20:23], s44 offen offset:128\n\t"
    "buffer_load_dwordx4 v148, s[24:27], s53 offen offset:2048  lds\n\t"
    "buffer_load_dwordx4 v[32:35], v139, s[20:23], s44 offen offset:128\n\t"
    "buffer_load_dwordx4 v149, s[24:27], s53 offen offset:3072  lds\n\t"
    "buffer_load_dwordx4 v[36:39], v140, s[20:23], s44 offen\n\t"
    "s_waitcnt lgkmcnt(0)\n\t"
    "s_waitcnt vmcnt(16)\n\t"
    "v_fma_mix_f32 v112, v4, v40, v52 op_sel_hi:[1,0,0]\n\t"
    "v_fma_mix_f32 v113, v5, v41, v53 op_sel_hi:[1,0,0]\n\t"
    "v_fma_mix_f32 v114, v6, v42, v54 op_sel_hi:[1,0,0]\n\t"
    "v_fma_mix_f32 v115, v7, v43, v55 op_sel_hi:[1,0,0]\n\t"
    "buffer_load_dwordx4 v[4:7], v136, s[20:23], s45 offen offset:0\n\t"
    "s_add_u32 m0, s74, 32768\n\t"
    "v_mul_f32 v112, v112, v64\n\t"
    "v_mul_f32 v113, v113, v64\n\t"
    "v_mul_f32 v114, v114, v64\n\t"
    "v_mul_f32 v115, v115, v64\n\t"
    "buffer_load_dwordx4 v142, s[24:27], s54 offen offset:0  lds\n\t"
    "s_waitcnt vmcnt(16)\n\t"
    "v_fma_mix_f32 v116, v8, v40, v52 op_sel_hi:[1,0,0]\n\t"
    "v_fma_mix_f32 v117, v9, v41, v53 op_sel_hi:[1,0,0]\n\t"
    "v_fma_mix_f32 v118, v10, v42, v54 op_sel_hi:[1,0,0]\n\t"
    "v_fma_mix_f32 v119, v11, v43, v55 op_sel_hi:[1,0,0]\n\t"
    "buffer_load_dwordx4 v[8:11], v137, s[20:23], s45 offen offset:0\n\t"
    "v_mul_f32 v116, v116, v65\n\t"
    "v_mul_f32 v117, v117, v65\n\t"
    "v_mul_f32 v118, v118, v65\n\t"
    "v_mul_f32 v119, v119, v65\n\t"
    "buffer_load_dwordx4 v143, s[24:27], s54 offen offset:1024  lds\n\t"
    "v_cvt_pk_bf16_f32 v120, v112, v116\n\t"
    "v_cvt_pk_bf16_f32 v122, v113, v117\n\t"
    "v_cvt_pk_bf16_f32 v124, v114, v118\n\t"
    "v_cvt_pk_bf16_f32 v126, v115, v119\n\t"
    "s_waitcnt vmcnt(16)\n\t"
    "v_fma_mix_f32 v112, v12, v40, v52 op_sel_hi:[1,0,0]\n\t"
    "v_fma_mix_f32 v113, v13, v41, v53 op_sel_hi:[1,0,0]\n\t"
    "v_fma_mix_f32 v114, v14, v42, v54 op_sel_hi:[1,0,0]\n\t"
    "v_fma_mix_f32 v115, v15, v43, v55 op_sel_hi:[1,0,0]\n\t"
    "buffer_load_dwordx4 v[12:15], v138, s[20:23], s45 offen offset:0\n\t"
    "v_mul_f32 v112, v112, v66\n\t"
    "v_mul_f32 v113, v113, v66\n\t"
    "v_mul_f32 v114, v114, v66\n\t"
    "v_mul_f32 v115, v115, v66\n\t"
    "buffer_load_dwordx4 v144, s[24:27], s54 offen offset:2048  lds\n\t"
    "s_waitcnt vmcnt(16)\n\t"
    "v_fma_mix_f32 v116, v16, v40, v52 op_sel_hi:[1,0,0]\n\t"
    "v_fma_mix_f32 v117, v17, v41, v53 op_sel_hi:[1,0,0]\n\t"
    "v_fma_mix_f32 v118, v18, v42, v54 op_sel_hi:[1,0,0]\n\t"
    "v_fma_mix_f32 v119, v19, v43, v55 op_sel_hi:[1,0,0]\n\t"
    "buffer_load_dwordx4 v[16:19], v139, s[20:23], s45 offen offset:0\n\t"
    "v_mul_f32 v116, v116, v67\n\t"
    "v_mul_f32 v117, v117, v67\n\t"
    "v_mul_f32 v118, v118, v67\n\t"
    "v_mul_f32 v119, v119, v67\n\t"
    "buffer_load_dwordx4 v145, s[24:27], s54 offen offset:3072  lds\n\t"
    "v_cvt_pk_bf16_f32 v121, v112, v116\n\t"
    "v_cvt_pk_bf16_f32 v123, v113, v117\n\t"
    "v_cvt_pk_bf16_f32 v125, v114, v118\n\t"
    "v_cvt_pk_bf16_f32 v127, v115, v119\n\t"
    "s_waitcnt vmcnt(16)\n\t"
    "v_fma_mix_f32 v112, v20, v44, v56 op_sel_hi:[1,0,0]\n\t"
    "v_fma_mix_f32 v113, v21, v45, v57 op_sel_hi:[1,0,0]\n\t"
    "v_fma_mix_f32 v114, v22, v46, v58 op_sel_hi:[1,0,0]\n\t"
    "v_fma_mix_f32 v115, v23, v47, v59 op_sel_hi:[1,0,0]\n\t"
    "buffer_load_dwordx4 v[20:23], v136, s[20:23], s45 offen offset:128\n\t"
    "s_add_u32 m0, s74, 36864\n\t"
    "v_mul_f32 v112, v112, v64\n\t"
    "v_mul_f32 v113, v113, v64\n\t"
    "v_mul_f32 v114, v114, v64\n\t"
    "v_mul_f32 v115, v115, v64\n\t"
    "buffer_load_dwordx4 v146, s[24:27], s54 offen offset:0  lds\n\t"
    "ds_write_b64 v150, v[120:121] offset:0\n\t"
    "s_waitcnt vmcnt(16)\n\t"
    "v_fma_mix_f32 v116, v24, v44, v56 op_sel_hi:[1,0,0]\n\t"
    "v_fma_mix_f32 v117, v25, v45, v57 op_sel_hi:[1,0,0]\n\t"
    "v_fma_mix_f32 v118, v26, v46, v58 op_sel_hi:[1,0,0]\n\t"
    "v_fma_mix_f32 v119, v27, v47, v59 op_sel_hi:[1,0,0]\n\t"
    "buffer_load_dwordx4 v[24:27], v137, s[20:23], s45 offen offset:128\n\t"
    "v_mul_f32 v116, v116, v65\n\t"
    "v_mul_f32 v117, v117, v65\n\t"
    "v_mul_f32 v118, v118, v65\n\t"
    "v_mul_f32 v119, v119, v65\n\t"
    "buffer_load_dwordx4 v147, s[24:27], s54 offen offset:1024  lds\n\t"
    "ds_write_b64 v150, v[122:123] offset:2064\n\t"
    "v_cvt_pk_bf16_f32 v128, v112, v116\n\t"
    "v_cvt_pk_bf16_f32 v130, v113, v117\n\t"
    "v_cvt_pk_bf16_f32 v132, v114, v118\n\t"
    "v_cvt_pk_bf16_f32 v134, v115, v119\n\t"
    "s_waitcnt vmcnt(16)\n\t"
    "v_fma_mix_f32 v112, v28, v44, v56 op_sel_hi:[1,0,0]\n\t"
    "v_fma_mix_f32 v113, v29, v45, v57 op_sel_hi:[1,0,0]\n\t"
    "v_fma_mix_f32 v114, v30, v46, v58 op_sel_hi:[1,0,0]\n\t"
    "v_fma_mix_f32 v115, v31, v47, v59 op_sel_hi:[1,0,0]\n\t"
    "buffer_load_dwordx4 v[28:31], v138, s[20:23], s45 offen offset:128\n\t"
    "v_mul_f32 v112, v112, v66\n\t"
    "v_mul_f32 v113, v113, v66\n\t"
    "v_mul_f32 v114, v114, v66\n\t"
    "v_mul_f32 v115, v115, v66\n\t"
    "ds_write_b64 v150, v[124:125] offset:4128\n\t"
    "s_waitcnt vmcnt(15)\n\t"
    "v_fma_mix_f32 v116, v32, v44, v56 op_sel_hi:[1,0,0]\n\t"
    "v_fma_mix_f32 v117, v33, v45, v57 op_sel_hi:[1,0,0]\n\t"
    "v_fma_mix_f32 v118, v34, v46, v58 op_sel_hi:[1,0,0]\n\t"
    "v_fma_mix_f32 v119, v35, v47, v59 op_sel_hi:[1,0,0]\n\t"
    "buffer_load_dwordx4 v[32:35], v139, s[20:23], s45 offen offset:128\n\t"
    "v_mul_f32 v116, v116, v67\n\t"
    "v_mul_f32 v117, v117, v67\n\t"
    "v_mul_f32 v118, v118, v67\n\t"
    "v_mul_f32 v119, v119, v67\n\t"
    "ds_write_b64 v150, v[126:127] offset:6192\n\t"
    "v_cvt_pk_bf16_f32 v129, v112, v116\n\t"
    "v_cvt_pk_bf16_f32 v131, v113, v117\n\t"
    "v_cvt_pk_bf16_f32 v133, v114, v118\n\t"
    "v_cvt_pk_bf16_f32 v135, v115, v119\n\t"
    "s_waitcnt vmcnt(14)\n\t"
    "ds_write_b64 v151, v[128:129] offset:0\n\t"
    "v_fma_mix_f32 v112, v36, v48, v60 op_sel_hi:[1,0,0]\n\t"
    "v_fma_mix_f32 v113, v37, v49, v61 op_sel_hi:[1,0,0]\n\t"
    "v_fma_mix_f32 v114, v38, v50, v62 op_sel_hi:[1,0,0]\n\t"
    "v_fma_mix_f32 v115, v39, v51, v63 op_sel_hi:[1,0,0]\n\t"
    "ds_write_b64 v151, v[130:131] offset:2064\n\t"
    "v_mul_f32 v112, v112, v68\n\t"
    "v_mul_f32 v113, v113, v68\n\t"
    "v_mul_f32 v114, v114, v68\n\t"
    "v_mul_f32 v115, v115, v68\n\t"
    "ds_write_b64 v151, v[132:133] offset:4128\n\t"
    "v_cvt_pk_bf16_f32 v116, v112, v113\n\t"
    "v_cvt_pk_bf16_f32 v117, v114, v115\n\t"
    "ds_write_b64 v151, v[134:135] offset:6192\n\t"
    "ds_write_b64 v152, v[116:117] offset:0\n\t"
    "buffer_load_dwordx4 v[36:39], v140, s[20:23], s45 offen\n\t"
    "buffer_load_dwordx4 v148, s[24:27], s54 offen offset:2048  lds\n\t"
    "buffer_load_dwordx4 v149, s[24:27], s54 offen offset:3072  lds\n\t"
    "s_waitcnt lgkmcnt(0)\n\t"
    "s_setprio 0\n\t"
    "s_barrier\n\t"
    "s_waitcnt vmcnt(16)\n\t"
    "v_fma_mix_f32 v112, v4, v40, v52 op_sel_hi:[1,0,0]\n\t"
    "v_fma_mix_f32 v113, v5, v41, v53 op_sel_hi:[1,0,0]\n\t"
    "v_fma_mix_f32 v114, v6, v42, v54 op_sel_hi:[1,0,0]\n\t"
    "v_fma_mix_f32 v115, v7, v43, v55 op_sel_hi:[1,0,0]\n\t"
    "buffer_load_dwordx4 v[4:7], v136, s[20:23], s46 offen offset:0\n\t"
    "s_add_u32 m0, s74, 65536\n\t"
    "v_mul_f32 v112, v112, v69\n\t"
    "v_mul_f32 v113, v113, v69\n\t"
    "v_mul_f32 v114, v114, v69\n\t"
    "v_mul_f32 v115, v115, v69\n\t"
    "buffer_load_dwordx4 v142, s[24:27], s55 offen offset:0  lds\n\t"
    "s_waitcnt vmcnt(16)\n\t"
    "v_fma_mix_f32 v116, v8, v40, v52 op_sel_hi:[1,0,0]\n\t"
    "v_fma_mix_f32 v117, v9, v41, v53 op_sel_hi:[1,0,0]\n\t"
    "v_fma_mix_f32 v118, v10, v42, v54 op_sel_hi:[1,0,0]\n\t"
    "v_fma_mix_f32 v119, v11, v43, v55 op_sel_hi:[1,0,0]\n\t"
    "buffer_load_dwordx4 v[8:11], v137, s[20:23], s46 offen offset:0\n\t"
    "v_mul_f32 v116, v116, v70\n\t"
    "v_mul_f32 v117, v117, v70\n\t"
    "v_mul_f32 v118, v118, v70\n\t"
    "v_mul_f32 v119, v119, v70\n\t"
    "buffer_load_dwordx4 v143, s[24:27], s55 offen offset:1024  lds\n\t"
    "v_cvt_pk_bf16_f32 v120, v112, v116\n\t"
    "v_cvt_pk_bf16_f32 v122, v113, v117\n\t"
    "v_cvt_pk_bf16_f32 v124, v114, v118\n\t"
    "v_cvt_pk_bf16_f32 v126, v115, v119\n\t"
    "s_waitcnt vmcnt(16)\n\t"
    "v_fma_mix_f32 v112, v12, v40, v52 op_sel_hi:[1,0,0]\n\t"
    "v_fma_mix_f32 v113, v13, v41, v53 op_sel_hi:[1,0,0]\n\t"
    "v_fma_mix_f32 v114, v14, v42, v54 op_sel_hi:[1,0,0]\n\t"
    "v_fma_mix_f32 v115, v15, v43, v55 op_sel_hi:[1,0,0]\n\t"
    "buffer_load_dwordx4 v[12:15], v138, s[20:23], s46 offen offset:0\n\t"
    "v_mul_f32 v112, v112, v71\n\t"
    "v_mul_f32 v113, v113, v71\n\t"
    "v_mul_f32 v114, v114, v71\n\t"
    "v_mul_f32 v115, v115, v71\n\t"
    "buffer_load_dwordx4 v144, s[24:27], s55 offen offset:2048  lds\n\t"
    "s_waitcnt vmcnt(16)\n\t"
    "v_fma_mix_f32 v116, v16, v40, v52 op_sel_hi:[1,0,0]\n\t"
    "v_fma_mix_f32 v117, v17, v41, v53 op_sel_hi:[1,0,0]\n\t"
    "v_fma_mix_f32 v118, v18, v42, v54 op_sel_hi:[1,0,0]\n\t"
    "v_fma_mix_f32 v119, v19, v43, v55 op_sel_hi:[1,0,0]\n\t"
    "buffer_load_dwordx4 v[16:19], v139, s[20:23], s46 offen offset:0\n\t"
    "v_mul_f32 v116, v116, v72\n\t"
    "v_mul_f32 v117, v117, v72\n\t"
    "v_mul_f32 v118, v118, v72\n\t"
    "v_mul_f32 v119, v119, v72\n\t"
    "buffer_load_dwordx4 v145, s[24:27], s55 offen offset:3072  lds\n\t"
    "v_cvt_pk_bf16_f32 v121, v112, v116\n\t"
    "v_cvt_pk_bf16_f32 v123, v113, v117\n\t"
    "v_cvt_pk_bf16_f32 v125, v114, v118\n\t"
    "v_cvt_pk_bf16_f32 v127, v115, v119\n\t"
    "s_waitcnt vmcnt(16)\n\t"
    "v_fma_mix_f32 v112, v20, v44, v56 op_sel_hi:[1,0,0]\n\t"
    "v_fma_mix_f32 v113, v21, v45, v57 op_sel_hi:[1,0,0]\n\t"
    "v_fma_mix_f32 v114, v22, v46, v58 op_sel_hi:[1,0,0]\n\t"
    "v_fma_mix_f32 v115, v23, v47, v59 op_sel_hi:[1,0,0]\n\t"
    "buffer_load_dwordx4 v[20:23], v136, s[20:23], s46 offen offset:128\n\t"
    "s_add_u32 m0, s74, 69632\n\t"
    "v_mul_f32 v112, v112, v69\n\t"
    "v_mul_f32 v113, v113, v69\n\t"
    "v_mul_f32 v114, v114, v69\n\t"
    "v_mul_f32 v115, v115, v69\n\t"
    "buffer_load_dwordx4 v146, s[24:27], s55 offen offset:0  lds\n\t"
    "ds_write_b64 v150, v[120:121] offset:18560\n\t"
    "s_waitcnt vmcnt(16)\n\t"
    "v_fma_mix_f32 v116, v24, v44, v56 op_sel_hi:[1,0,0]\n\t"
    "v_fma_mix_f32 v117, v25, v45, v57 op_sel_hi:[1,0,0]\n\t"
    "v_fma_mix_f32 v118, v26, v46, v58 op_sel_hi:[1,0,0]\n\t"
    "v_fma_mix_f32 v119, v27, v47, v59 op_sel_hi:[1,0,0]\n\t"
    "buffer_load_dwordx4 v[24:27], v137, s[20:23], s46 offen offset:128\n\t"
    "v_mul_f32 v116, v116, v70\n\t"
    "v_mul_f32 v117, v117, v70\n\t"
    "v_mul_f32 v118, v118, v70\n\t"
    "v_mul_f32 v119, v119, v70\n\t"
    "buffer_load_dwordx4 v147, s[24:27], s55 offen offset:1024  lds\n\t"
    "ds_write_b64 v150, v[122:123] offset:20624\n\t"
    "v_cvt_pk_bf16_f32 v128, v112, v116\n\t"
    "v_cvt_pk_bf16_f32 v130, v113, v117\n\t"
    "v_cvt_pk_bf16_f32 v132, v114, v118\n\t"
    "v_cvt_pk_bf16_f32 v134, v115, v119\n\t"
    "s_waitcnt vmcnt(16)\n\t"
    "v_fma_mix_f32 v112, v28, v44, v56 op_sel_hi:[1,0,0]\n\t"
    "v_fma_mix_f32 v113, v29, v45, v57 op_sel_hi:[1,0,0]\n\t"
    "v_fma_mix_f32 v114, v30, v46, v58 op_sel_hi:[1,0,0]\n\t"
    "v_fma_mix_f32 v115, v31, v47, v59 op_sel_hi:[1,0,0]\n\t"
    "buffer_load_dwordx4 v[28:31], v138, s[20:23], s46 offen offset:128\n\t"
    "v_mul_f32 v112, v112, v71\n\t"
    "v_mul_f32 v113, v113, v71\n\t"
    "v_mul_f32 v114, v114, v71\n\t"
    "v_mul_f32 v115, v115, v71\n\t"
    "ds_write_b64 v150, v[124:125] offset:22688\n\t"
    "s_waitcnt vmcnt(16)\n\t"
    "v_fma_mix_f32 v116, v32, v44, v56 op_sel_hi:[1,0,0]\n\t"
    "v_fma_mix_f32 v117, v33, v45, v57 op_sel_hi:[1,0,0]\n\t"
    "v_fma_mix_f32 v118, v34, v46, v58 op_sel_hi:[1,0,0]\n\t"
    "v_fma_mix_f32 v119, v35, v47, v59 op_sel_hi:[1,0,0]\n\t"
    "buffer_load_dwordx4 v[32:35], v139, s[20:23], s46 offen offset:128\n\t"
    "v_mul_f32 v116, v116, v72\n\t"
    "v_mul_f32 v117, v117, v72\n\t"
    "v_mul_f32 v118, v118, v72\n\t"
    "v_mul_f32 v119, v119, v72\n\t"
    "ds_write_b64 v150, v[126:127] offset:24752\n\t"
    "v_cvt_pk_bf16_f32 v129, v112, v116\n\t"
    "v_cvt_pk_bf16_f32 v131, v113, v117\n\t"
    "v_cvt_pk_bf16_f32 v133, v114, v118\n\t"
    "v_cvt_pk_bf16_f32 v135, v115, v119\n\t"
    "s_waitcnt vmcnt(16)\n\t"
    "ds_write_b64 v151, v[128:129] offset:18560\n\t"
    "v_fma_mix_f32 v112, v36, v48, v60 op_sel_hi:[1,0,0]\n\t"
    "v_fma_mix_f32 v113, v37, v49, v61 op_sel_hi:[1,0,0]\n\t"
    "v_fma_mix_f32 v114, v38, v50, v62 op_sel_hi:[1,0,0]\n\t"
    "v_fma_mix_f32 v115, v39, v51, v63 op_sel_hi:[1,0,0]\n\t"
    "ds_write_b64 v151, v[130:131] offset:20624\n\t"
    "v_mul_f32 v112, v112, v73\n\t"
    "v_mul_f32 v113, v113, v73\n\t"
    "v_mul_f32 v114, v114, v73\n\t"
    "v_mul_f32 v115, v115, v73\n\t"
    "ds_write_b64 v151, v[132:133] offset:22688\n\t"
    "v_cvt_pk_bf16_f32 v116, v112, v113\n\t"
    "v_cvt_pk_bf16_f32 v117, v114, v115\n\t"
    "ds_write_b64 v151, v[134:135] offset:24752\n\t"
    "ds_write_b64 v152, v[116:117] offset:18560\n\t"
    "buffer_load_dwordx4 v[36:39], v140, s[20:23], s46 offen\n\t"
    "buffer_load_dwordx4 v148, s[24:27], s55 offen offset:2048  lds\n\t"
    "buffer_load_dwordx4 v149, s[24:27], s55 offen offset:3072  lds\n\t"
    "s_waitcnt vmcnt(17) lgkmcnt(0)\n\t"
    "s_barrier\n\t"
    "s_waitcnt vmcnt(16)\n\t"
    "v_fma_mix_f32 v112, v4, v40, v52 op_sel_hi:[1,0,0]\n\t"
    "v_fma_mix_f32 v113, v5, v41, v53 op_sel_hi:[1,0,0]\n\t"
    "v_fma_mix_f32 v114, v6, v42, v54 op_sel_hi:[1,0,0]\n\t"
    "v_fma_mix_f32 v115, v7, v43, v55 op_sel_hi:[1,0,0]\n\t"
    "buffer_load_dwordx4 v[4:7], v136, s[20:23], s47 offen offset:0\n\t"
    "s_add_u32 m0, s74, 0\n\t"
    "v_mul_f32 v112, v112, v74\n\t"
    "v_mul_f32 v113, v113, v74\n\t"
    "v_mul_f32 v114, v114, v74\n\t"
    "v_mul_f32 v115, v115, v74\n\t"
    "buffer_load_dwordx4 v142, s[24:27], s56 offen offset:0  lds\n\t"
    "s_waitcnt vmcnt(16)\n\t"
    "v_fma_mix_f32 v116, v8, v40, v52 op_sel_hi:[1,0,0]\n\t"
    "v_fma_mix_f32 v117, v9, v41, v53 op_sel_hi:[1,0,0]\n\t"
    "v_fma_mix_f32 v118, v10, v42, v54 op_sel_hi:[1,0,0]\n\t"
    "v_fma_mix_f32 v119, v11, v43, v55 op_sel_hi:[1,0,0]\n\t"
    "buffer_load_dwordx4 v[8:11], v137, s[20:23], s47 offen offset:0\n\t"
    "v_mul_f32 v116, v116, v75\n\t"
    "v_mul_f32 v117, v117, v75\n\t"
    "v_mul_f32 v118, v118, v75\n\t"
    "v_mul_f32 v119, v119, v75\n\t"
    "buffer_load_dwordx4 v143, s[24:27], s56 offen offset:1024  lds\n\t"
    "v_cvt_pk_bf16_f32 v120, v112, v116\n\t"
    "v_cvt_pk_bf16_f32 v122, v113, v117\n\t"
    "v_cvt_pk_bf16_f32 v124, v114, v118\n\t"
    "v_cvt_pk_bf16_f32 v126, v115, v119\n\t"
    "s_waitcnt vmcnt(16)\n\t"
    "v_fma_mix_f32 v112, v12, v40, v52 op_sel_hi:[1,0,0]\n\t"
    "v_fma_mix_f32 v113, v13, v41, v53 op_sel_hi:[1,0,0]\n\t"
    "v_fma_mix_f32 v114, v14, v42, v54 op_sel_hi:[1,0,0]\n\t"
    "v_fma_mix_f32 v115, v15, v43, v55 op_sel_hi:[1,0,0]\n\t"
    "buffer_load_dwordx4 v[12:15], v138, s[20:23], s47 offen offset:0\n\t"
    "v_mul_f32 v112, v112, v76\n\t"
    "v_mul_f32 v113, v113, v76\n\t"
    "v_mul_f32 v114, v114, v76\n\t"
    "v_mul_f32 v115, v115, v76\n\t"
    "buffer_load_dwordx4 v144, s[24:27], s56 offen offset:2048  lds\n\t"
    "s_waitcnt vmcnt(16)\n\t"
    "v_fma_mix_f32 v116, v16, v40, v52 op_sel_hi:[1,0,0]\n\t"
    "v_fma_mix_f32 v117, v17, v41, v53 op_sel_hi:[1,0,0]\n\t"
    "v_fma_mix_f32 v118, v18, v42, v54 op_sel_hi:[1,0,0]\n\t"
    "v_fma_mix_f32 v119, v19, v43, v55 op_sel_hi:[1,0,0]\n\t"
    "buffer_load_dwordx4 v[16:19], v139, s[20:23], s47 offen offset:0\n\t"
    "v_mul_f32 v116, v116, v77\n\t"
    "v_mul_f32 v117, v117, v77\n\t"
    "v_mul_f32 v118, v118, v77\n\t"
    "v_mul_f32 v119, v119, v77\n\t"
    "buffer_load_dwordx4 v145, s[24:27], s56 offen offset:3072  lds\n\t"
    "v_cvt_pk_bf16_f32 v121, v112, v116\n\t"
    "v_cvt_pk_bf16_f32 v123, v113, v117\n\t"
    "v_cvt_pk_bf16_f32 v125, v114, v118\n\t"
    "v_cvt_pk_bf16_f32 v127, v115, v119\n\t"
    "s_waitcnt vmcnt(16)\n\t"
    "v_fma_mix_f32 v112, v20, v44, v56 op_sel_hi:[1,0,0]\n\t"
    "v_fma_mix_f32 v113, v21, v45, v57 op_sel_hi:[1,0,0]\n\t"
    "v_fma_mix_f32 v114, v22, v46, v58 op_sel_hi:[1,0,0]\n\t"
    "v_fma_mix_f32 v115, v23, v47, v59 op_sel_hi:[1,0,0]\n\t"
    "buffer_load_dwordx4 v[20:23], v136, s[20:23], s47 offen offset:128\n\t"
    "s_add_u32 m0, s74, 4096\n\t"
    "v_mul_f32 v112, v112, v74\n\t"
    "v_mul_f32 v113, v113, v74\n\t"
    "v_mul_f32 v114, v114, v74\n\t"
    "v_mul_f32 v115, v115, v74\n\t"
    "buffer_load_dwordx4 v146, s[24:27], s56 offen offset:0  lds\n\t"
    "ds_write_b64 v150, v[120:121] offset:0\n\t"
    "s_waitcnt vmcnt(16)\n\t"
    "v_fma_mix_f32 v116, v24, v44, v56 op_sel_hi:[1,0,0]\n\t"
    "v_fma_mix_f32 v117, v25, v45, v57 op_sel_hi:[1,0,0]\n\t"
    "v_fma_mix_f32 v118, v26, v46, v58 op_sel_hi:[1,0,0]\n\t"
    "v_fma_mix_f32 v119, v27, v47, v59 op_sel_hi:[1,0,0]\n\t"
    "buffer_load_dwordx4 v[24:27], v137, s[20:23], s47 offen offset:128\n\t"
    "v_mul_f32 v116, v116, v75\n\t"
    "v_mul_f32 v117, v117, v75\n\t"
    "v_mul_f32 v118, v118, v75\n\t"
    "v_mul_f32 v119, v119, v75\n\t"
    "buffer_load_dwordx4 v147, s[24:27], s56 offen offset:1024  lds\n\t"
    "ds_write_b64 v150, v[122:123] offset:2064\n\t"
    "v_cvt_pk_bf16_f32 v128, v112, v116\n\t"
    "v_cvt_pk_bf16_f32 v130, v113, v117\n\t"
    "v_cvt_pk_bf16_f32 v132, v114, v118\n\t"
    "v_cvt_pk_bf16_f32 v134, v115, v119\n\t"
    "s_waitcnt vmcnt(16)\n\t"
    "v_fma_mix_f32 v112, v28, v44, v56 op_sel_hi:[1,0,0]\n\t"
    "v_fma_mix_f32 v113, v29, v45, v57 op_sel_hi:[1,0,0]\n\t"
    "v_fma_mix_f32 v114, v30, v46, v58 op_sel_hi:[1,0,0]\n\t"
    "v_fma_mix_f32 v115, v31, v47, v59 op_sel_hi:[1,0,0]\n\t"
    "buffer_load_dwordx4 v[28:31], v138, s[20:23], s47 offen offset:128\n\t"
    "v_mul_f32 v112, v112, v76\n\t"
    "v_mul_f32 v113, v113, v76\n\t"
    "v_mul_f32 v114, v114, v76\n\t"
    "v_mul_f32 v115, v115, v76\n\t"
    "ds_write_b64 v150, v[124:125] offset:4128\n\t"
    "s_waitcnt vmcnt(16)\n\t"
    "v_fma_mix_f32 v116, v32, v44, v56 op_sel_hi:[1,0,0]\n\t"
    "v_fma_mix_f32 v117, v33, v45, v57 op_sel_hi:[1,0,0]\n\t"
    "v_fma_mix_f32 v118, v34, v46, v58 op_sel_hi:[1,0,0]\n\t"
    "v_fma_mix_f32 v119, v35, v47, v59 op_sel_hi:[1,0,0]\n\t"
    "buffer_load_dwordx4 v[32:35], v139, s[20:23], s47 offen offset:128\n\t"
    "v_mul_f32 v116, v116, v77\n\t"
    "v_mul_f32 v117, v117, v77\n\t"
    "v_mul_f32 v118, v118, v77\n\t"
    "v_mul_f32 v119, v119, v77\n\t"
    "ds_write_b64 v150, v[126:127] offset:6192\n\t"
    "v_cvt_pk_bf16_f32 v129, v112, v116\n\t"
    "v_cvt_pk_bf16_f32 v131, v113, v117\n\t"
    "v_cvt_pk_bf16_f32 v133, v114, v118\n\t"
    "v_cvt_pk_bf16_f32 v135, v115, v119\n\t"
    "s_waitcnt vmcnt(16)\n\t"
    "ds_write_b64 v151, v[128:129] offset:0\n\t"
    "v_fma_mix_f32 v112, v36, v48, v60 op_sel_hi:[1,0,0]\n\t"
    "v_fma_mix_f32 v113, v37, v49, v61 op_sel_hi:[1,0,0]\n\t"
    "v_fma_mix_f32 v114, v38, v50, v62 op_sel_hi:[1,0,0]\n\t"
    "v_fma_mix_f32 v115, v39, v51, v63 op_sel_hi:[1,0,0]\n\t"
    "ds_write_b64 v151, v[130:131] offset:2064\n\t"
    "v_mul_f32 v112, v112, v78\n\t"
    "v_mul_f32 v113, v113, v78\n\t"
    "v_mul_f32 v114, v114, v78\n\t"
    "v_mul_f32 v115, v115, v78\n\t"
    "ds_write_b64 v151, v[132:133] offset:4128\n\t"
    "v_cvt_pk_bf16_f32 v116, v112, v113\n\t"
    "v_cvt_pk_bf16_f32 v117, v114, v115\n\t"
    "ds_write_b64 v151, v[134:135] offset:6192\n\t"
    "ds_write_b64 v152, v[116:117] offset:0\n\t"
    "buffer_load_dwordx4 v[36:39], v140, s[20:23], s47 offen\n\t"
    "buffer_load_dwordx4 v148, s[24:27], s56 offen offset:2048  lds\n\t"
    "buffer_load_dwordx4 v149, s[24:27], s56 offen offset:3072  lds\n\t"
    "s_waitcnt vmcnt(17) lgkmcnt(0)\n\t"
    "s_barrier\n\t"
    "s_waitcnt vmcnt(16)\n\t"
    "v_fma_mix_f32 v112, v4, v40, v52 op_sel_hi:[1,0,0]\n\t"
    "v_fma_mix_f32 v113, v5, v41, v53 op_sel_hi:[1,0,0]\n\t"
    "v_fma_mix_f32 v114, v6, v42, v54 op_sel_hi:[1,0,0]\n\t"
    "v_fma_mix_f32 v115, v7, v43, v55 op_sel_hi:[1,0,0]\n\t"
    "buffer_load_dwordx4 v[4:7], v136, s[20:23], s48 offen offset:0\n\t"
    "s_add_u32 m0, s74, 32768\n\t"
    "v_mul_f32 v112, v112, v79\n\t"
    "v_mul_f32 v113, v113, v79\n\t"
    "v_mul_f32 v114, v114, v79\n\t"
    "v_mul_f32 v115, v115, v79\n\t"
    "buffer_load_dwordx4 v142, s[24:27], s57 offen offset:0  lds\n\t"
    "s_waitcnt vmcnt(16)\n\t"
    "v_fma_mix_f32 v116, v8, v40, v52 op_sel_hi:[1,0,0]\n\t"
    "v_fma_mix_f32 v117, v9, v41, v53 op_sel_hi:[1,0,0]\n\t"
    "v_fma_mix_f32 v118, v10, v42, v54 op_sel_hi:[1,0,0]\n\t"
    "v_fma_mix_f32 v119, v11, v43, v55 op_sel_hi:[1,0,0]\n\t"
    "buffer_load_dwordx4 v[8:11], v137, s[20:23], s48 offen offset:0\n\t"
    "v_mul_f32 v116, v116, v80\n\t"
    "v_mul_f32 v117, v117, v80\n\t"
    "v_mul_f32 v118, v118, v80\n\t"
    "v_mul_f32 v119, v119, v80\n\t"
    "buffer_load_dwordx4 v143, s[24:27], s57 offen offset:1024  lds\n\t"
    "v_cvt_pk_bf16_f32 v120, v112, v116\n\t"
    "v_cvt_pk_bf16_f32 v122, v113, v117\n\t"
    "v_cvt_pk_bf16_f32 v124, v114, v118\n\t"
    "v_cvt_pk_bf16_f32 v126, v115, v119\n\t"
    "s_waitcnt vmcnt(16)\n\t"
    "v_fma_mix_f32 v112, v12, v40, v52 op_sel_hi:[1,0,0]\n\t"
    "v_fma_mix_f32 v113, v13, v41, v53 op_sel_hi:[1,0,0]\n\t"
    "v_fma_mix_f32 v114, v14, v42, v54 op_sel_hi:[1,0,0]\n\t"
    "v_fma_mix_f32 v115, v15, v43, v55 op_sel_hi:[1,0,0]\n\t"
    "buffer_load_dwordx4 v[12:15], v138, s[20:23], s48 offen offset:0\n\t"
    "v_mul_f32 v112, v112, v81\n\t"
    "v_mul_f32 v113, v113, v81\n\t"
    "v_mul_f32 v114, v114, v81\n\t"
    "v_mul_f32 v115, v115, v81\n\t"
    "buffer_load_dwordx4 v144, s[24:27], s57 offen offset:2048  lds\n\t"
    "s_waitcnt vmcnt(16)\n\t"
    "v_fma_mix_f32 v116, v16, v40, v52 op_sel_hi:[1,0,0]\n\t"
    "v_fma_mix_f32 v117, v17, v41, v53 op_sel_hi:[1,0,0]\n\t"
    "v_fma_mix_f32 v118, v18, v42, v54 op_sel_hi:[1,0,0]\n\t"
    "v_fma_mix_f32 v119, v19, v43, v55 op_sel_hi:[1,0,0]\n\t"
    "buffer_load_dwordx4 v[16:19], v139, s[20:23], s48 offen offset:0\n\t"
    "v_mul_f32 v116, v116, v82\n\t"
    "v_mul_f32 v117, v117, v82\n\t"
    "v_mul_f32 v118, v118, v82\n\t"
    "v_mul_f32 v119, v119, v82\n\t"
    "buffer_load_dwordx4 v145, s[24:27], s57 offen offset:3072  lds\n\t"
    "v_cvt_pk_bf16_f32 v121, v112, v116\n\t"
    "v_cvt_pk_bf16_f32 v123, v113, v117\n\t"
    "v_cvt_pk_bf16_f32 v125, v114, v118\n\t"
    "v_cvt_pk_bf16_f32 v127, v115, v119\n\t"
    "s_waitcnt vmcnt(16)\n\t"
    "v_fma_mix_f32 v112, v20, v44, v56 op_sel_hi:[1,0,0]\n\t"
    "v_fma_mix_f32 v113, v21, v45, v57 op_sel_hi:[1,0,0]\n\t"
    "v_fma_mix_f32 v114, v22, v46, v58 op_sel_hi:[1,0,0]\n\t"
    "v_fma_mix_f32 v115, v23, v47, v59 op_sel_hi:[1,0,0]\n\t"
    "buffer_load_dwordx4 v[20:23], v136, s[20:23], s48 offen offset:128\n\t"
    "s_add_u32 m0, s74, 36864\n\t"
    "v_mul_f32 v112, v112, v79\n\t"
    "v_mul_f32 v113, v113, v79\n\t"
    "v_mul_f32 v114, v114, v79\n\t"
    "v_mul_f32 v115, v115, v79\n\t"
    "buffer_load_dwordx4 v146, s[24:27], s57 offen offset:0  lds\n\t"
    "ds_write_b64 v150, v[120:121] offset:18560\n\t"
    "s_waitcnt vmcnt(16)\n\t"
    "v_fma_mix_f32 v116, v24, v44, v56 op_sel_hi:[1,0,0]\n\t"
    "v_fma_mix_f32 v117, v25, v45, v57 op_sel_hi:[1,0,0]\n\t"
    "v_fma_mix_f32 v118, v26, v46, v58 op_sel_hi:[1,0,0]\n\t"
    "v_fma_mix_f32 v119, v27, v47, v59 op_sel_hi:[1,0,0]\n\t"
    "buffer_load_dwordx4 v[24:27], v137, s[20:23], s48 offen offset:128\n\t"
    "v_mul_f32 v116, v116, v80\n\t"
    "v_mul_f32 v117, v117, v80\n\t"
    "v_mul_f32 v118, v118, v80\n\t"
    "v_mul_f32 v119, v119, v80\n\t"
    "buffer_load_dwordx4 v147, s[24:27], s57 offen offset:1024  lds\n\t"
    "ds_write_b64 v150, v[122:123] offset:20624\n\t"
    "v_cvt_pk_bf16_f32 v128, v112, v116\n\t"
    "v_cvt_pk_bf16_f32 v130, v113, v117\n\t"
    "v_cvt_pk_bf16_f32 v132, v114, v118\n\t"
    "v_cvt_pk_bf16_f32 v134, v115, v119\n\t"
    "s_waitcnt vmcnt(16)\n\t"
    "v_fma_mix_f32 v112, v28, v44, v56 op_sel_hi:[1,0,0]\n\t"
    "v_fma_mix_f32 v113, v29, v45, v57 op_sel_hi:[1,0,0]\n\t"
    "v_fma_mix_f32 v114, v30, v46, v58 op_sel_hi:[1,0,0]\n\t"
    "v_fma_mix_f32 v115, v31, v47, v59 op_sel_hi:[1,0,0]\n\t"
    "buffer_load_dwordx4 v[28:31], v138, s[20:23], s48 offen offset:128\n\t"
    "v_mul_f32 v112, v112, v81\n\t"
    "v_mul_f32 v113, v113, v81\n\t"
    "v_mul_f32 v114, v114, v81\n\t"
    "v_mul_f32 v115, v115, v81\n\t"
    "ds_write_b64 v150, v[124:125] offset:22688\n\t"
    "s_waitcnt vmcnt(16)\n\t"
    "v_fma_mix_f32 v116, v32, v44, v56 op_sel_hi:[1,0,0]\n\t"
    "v_fma_mix_f32 v117, v33, v45, v57 op_sel_hi:[1,0,0]\n\t"
    "v_fma_mix_f32 v118, v34, v46, v58 op_sel_hi:[1,0,0]\n\t"
    "v_fma_mix_f32 v119, v35, v47, v59 op_sel_hi:[1,0,0]\n\t"
    "buffer_load_dwordx4 v[32:35], v139, s[20:23], s48 offen offset:128\n\t"
    "v_mul_f32 v116, v116, v82\n\t"
    "v_mul_f32 v117, v117, v82\n\t"
    "v_mul_f32 v118, v118, v82\n\t"
    "v_mul_f32 v119, v119, v82\n\t"
    "ds_write_b64 v150, v[126:127] offset:24752\n\t"
    "v_cvt_pk_bf16_f32 v129, v112, v116\n\t"
    "v_cvt_pk_bf16_f32 v131, v113, v117\n\t"
    "v_cvt_pk_bf16_f32 v133, v114, v118\n\t"
    "v_cvt_pk_bf16_f32 v135, v115, v119\n\t"
    "s_waitcnt vmcnt(16)\n\t"
    "ds_write_b64 v151, v[128:129] offset:18560\n\t"
    "v_fma_mix_f32 v112, v36, v48, v60 op_sel_hi:[1,0,0]\n\t"
    "v_fma_mix_f32 v113, v37, v49, v61 op_sel_hi:[1,0,0]\n\t"
    "v_fma_mix_f32 v114, v38, v50, v62 op_sel_hi:[1,0,0]\n\t"
    "v_fma_mix_f32 v115, v39, v51, v63 op_sel_hi:[1,0,0]\n\t"
    "ds_write_b64 v151, v[130:131] offset:20624\n\t"
    "v_mul_f32 v112, v112, v83\n\t"
    "v_mul_f32 v113, v113, v83\n\t"
    "v_mul_f32 v114, v114, v83\n\t"
    "v_mul_f32 v115, v115, v83\n\t"
    "ds_write_b64 v151, v[132:133] offset:22688\n\t"
    "v_cvt_pk_bf16_f32 v116, v112, v113\n\t"
    "v_cvt_pk_bf16_f32 v117, v114, v115\n\t"
    "ds_write_b64 v151, v[134:135] offset:24752\n\t"
    "ds_write_b64 v152, v[116:117] offset:18560\n\t"
    "buffer_load_dwordx4 v[36:39], v140, s[20:23], s48 offen\n\t"
    "buffer_load_dwordx4 v148, s[24:27], s57 offen offset:2048  lds\n\t"
    "buffer_load_dwordx4 v149, s[24:27], s57 offen offset:3072  lds\n\t"
    "s_waitcnt vmcnt(17) lgkmcnt(0)\n\t"
    "s_barrier\n\t"
    "s_waitcnt vmcnt(16)\n\t"
    "v_fma_mix_f32 v112, v4, v40, v52 op_sel_hi:[1,0,0]\n\t"
    "v_fma_mix_f32 v113, v5, v41, v53 op_sel_hi:[1,0,0]\n\t"
    "v_fma_mix_f32 v114, v6, v42, v54 op_sel_hi:[1,0,0]\n\t"
    "v_fma_mix_f32 v115, v7, v43, v55 op_sel_hi:[1,0,0]\n\t"
    "buffer_load_dwordx4 v[4:7], v136, s[20:23], s49 offen offset:0\n\t"
    "s_add_u32 m0, s74, 65536\n\t"
    "v_mul_f32 v112, v112, v84\n\t"
    "v_mul_f32 v113, v113, v84\n\t"
    "v_mul_f32 v114, v114, v84\n\t"
    "v_mul_f32 v115, v115, v84\n\t"
    "buffer_load_dwordx4 v142, s[24:27], s58 offen offset:0  lds\n\t"
    "s_waitcnt vmcnt(16)\n\t"
    "v_fma_mix_f32 v116, v8, v40, v52 op_sel_hi:[1,0,0]\n\t"
    "v_fma_mix_f32 v117, v9, v41, v53 op_sel_hi:[1,0,0]\n\t"
    "v_fma_mix_f32 v118, v10, v42, v54 op_sel_hi:[1,0,0]\n\t"
    "v_fma_mix_f32 v119, v11, v43, v55 op_sel_hi:[1,0,0]\n\t"
    "buffer_load_dwordx4 v[8:11], v137, s[20:23], s49 offen offset:0\n\t"
    "v_mul_f32 v116, v116, v85\n\t"
    "v_mul_f32 v117, v117, v85\n\t"
    "v_mul_f32 v118, v118, v85\n\t"
    "v_mul_f32 v119, v119, v85\n\t"
    "buffer_load_dwordx4 v143, s[24:27], s58 offen offset:1024  lds\n\t"
    "v_cvt_pk_bf16_f32 v120, v112, v116\n\t"
    "v_cvt_pk_bf16_f32 v122, v113, v117\n\t"
    "v_cvt_pk_bf16_f32 v124, v114, v118\n\t"
    "v_cvt_pk_bf16_f32 v126, v115, v119\n\t"
    "s_waitcnt vmcnt(16)\n\t"
    "v_fma_mix_f32 v112, v12, v40, v52 op_sel_hi:[1,0,0]\n\t"
    "v_fma_mix_f32 v113, v13, v41, v53 op_sel_hi:[1,0,0]\n\t"
    "v_fma_mix_f32 v114, v14, v42, v54 op_sel_hi:[1,0,0]\n\t"
    "v_fma_mix_f32 v115, v15, v43, v55 op_sel_hi:[1,0,0]\n\t"
    "buffer_load_dwordx4 v[12:15], v138, s[20:23], s49 offen offset:0\n\t"
    "v_mul_f32 v112, v112, v86\n\t"
    "v_mul_f32 v113, v113, v86\n\t"
    "v_mul_f32 v114, v114, v86\n\t"
    "v_mul_f32 v115, v115, v86\n\t"
    "buffer_load_dwordx4 v144, s[24:27], s58 offen offset:2048  lds\n\t"
    "s_waitcnt vmcnt(16)\n\t"
    "v_fma_mix_f32 v116, v16, v40, v52 op_sel_hi:[1,0,0]\n\t"
    "v_fma_mix_f32 v117, v17, v41, v53 op_sel_hi:[1,0,0]\n\t"
    "v_fma_mix_f32 v118, v18, v42, v54 op_sel_hi:[1,0,0]\n\t"
    "v_fma_mix_f32 v119, v19, v43, v55 op_sel_hi:[1,0,0]\n\t"
    "buffer_load_dwordx4 v[16:19], v139, s[20:23], s49 offen offset:0\n\t"
    "v_mul_f32 v116, v116, v87\n\t"
    "v_mul_f32 v117, v117, v87\n\t"
    "v_mul_f32 v118, v118, v87\n\t"
    "v_mul_f32 v119, v119, v87\n\t"
    "buffer_load_dwordx4 v145, s[24:27], s58 offen offset:3072  lds\n\t"
    "v_cvt_pk_bf16_f32 v121, v112, v116\n\t"
    "v_cvt_pk_bf16_f32 v123, v113, v117\n\t"
    "v_cvt_pk_bf16_f32 v125, v114, v118\n\t"
    "v_cvt_pk_bf16_f32 v127, v115, v119\n\t"
    "s_waitcnt vmcnt(16)\n\t"
    "v_fma_mix_f32 v112, v20, v44, v56 op_sel_hi:[1,0,0]\n\t"
    "v_fma_mix_f32 v113, v21, v45, v57 op_sel_hi:[1,0,0]\n\t"
    "v_fma_mix_f32 v114, v22, v46, v58 op_sel_hi:[1,0,0]\n\t"
    "v_fma_mix_f32 v115, v23, v47, v59 op_sel_hi:[1,0,0]\n\t"
    "buffer_load_dwordx4 v[20:23], v136, s[20:23], s49 offen offset:128\n\t"
    "s_add_u32 m0, s74, 69632\n\t"
    "v_mul_f32 v112, v112, v84\n\t"
    "v_mul_f32 v113, v113, v84\n\t"
    "v_mul_f32 v114, v114, v84\n\t"
    "v_mul_f32 v115, v115, v84\n\t"
    "buffer_load_dwordx4 v146, s[24:27], s58 offen offset:0  lds\n\t"
    "ds_write_b64 v150, v[120:121] offset:0\n\t"
    "s_waitcnt vmcnt(16)\n\t"
    "v_fma_mix_f32 v116, v24, v44, v56 op_sel_hi:[1,0,0]\n\t"
    "v_fma_mix_f32 v117, v25, v45, v57 op_sel_hi:[1,0,0]\n\t"
    "v_fma_mix_f32 v118, v26, v46, v58 op_sel_hi:[1,0,0]\n\t"
    "v_fma_mix_f32 v119, v27, v47, v59 op_sel_hi:[1,0,0]\n\t"
    "buffer_load_dwordx4 v[24:27], v137, s[20:23], s49 offen offset:128\n\t"
    "v_mul_f32 v116, v116, v85\n\t"
    "v_mul_f32 v117, v117, v85\n\t"
    "v_mul_f32 v118, v118, v85\n\t"
    "v_mul_f32 v119, v119, v85\n\t"
    "buffer_load_dwordx4 v147, s[24:27], s58 offen offset:1024  lds\n\t"
    "ds_write_b64 v150, v[122:123] offset:2064\n\t"
    "v_cvt_pk_bf16_f32 v128, v112, v116\n\t"
    "v_cvt_pk_bf16_f32 v130, v113, v117\n\t"
    "v_cvt_pk_bf16_f32 v132, v114, v118\n\t"
    "v_cvt_pk_bf16_f32 v134, v115, v119\n\t"
    "s_waitcnt vmcnt(16)\n\t"
    "v_fma_mix_f32 v112, v28, v44, v56 op_sel_hi:[1,0,0]\n\t"
    "v_fma_mix_f32 v113, v29, v45, v57 op_sel_hi:[1,0,0]\n\t"
    "v_fma_mix_f32 v114, v30, v46, v58 op_sel_hi:[1,0,0]\n\t"
    "v_fma_mix_f32 v115, v31, v47, v59 op_sel_hi:[1,0,0]\n\t"
    "buffer_load_dwordx4 v[28:31], v138, s[20:23], s49 offen offset:128\n\t"
    "v_mul_f32 v112, v112, v86\n\t"
    "v_mul_f32 v113, v113, v86\n\t"
    "v_mul_f32 v114, v114, v86\n\t"
    "v_mul_f32 v115, v115, v86\n\t"
    "ds_write_b64 v150, v[124:125] offset:4128\n\t"
    "s_waitcnt vmcnt(16)\n\t"
    "v_fma_mix_f32 v116, v32, v44, v56 op_sel_hi:[1,0,0]\n\t"
    "v_fma_mix_f32 v117, v33, v45, v57 op_sel_hi:[1,0,0]\n\t"
    "v_fma_mix_f32 v118, v34, v46, v58 op_sel_hi:[1,0,0]\n\t"
    "v_fma_mix_f32 v119, v35, v47, v59 op_sel_hi:[1,0,0]\n\t"
    "buffer_load_dwordx4 v[32:35], v139, s[20:23], s49 offen offset:128\n\t"
    "v_mul_f32 v116, v116, v87\n\t"
    "v_mul_f32 v117, v117, v87\n\t"
    "v_mul_f32 v118, v118, v87\n\t"
    "v_mul_f32 v119, v119, v87\n\t"
    "ds_write_b64 v150, v[126:127] offset:6192\n\t"
    "v_cvt_pk_bf16_f32 v129, v112, v116\n\t"
    "v_cvt_pk_bf16_f32 v131, v113, v117\n\t"
    "v_cvt_pk_bf16_f32 v133, v114, v118\n\t"
    "v_cvt_pk_bf16_f32 v135, v115, v119\n\t"
    "s_waitcnt vmcnt(16)\n\t"
    "ds_write_b64 v151, v[128:129] offset:0\n\t"
    "v_fma_mix_f32 v112, v36, v48, v60 op_sel_hi:[1,0,0]\n\t"
    "v_fma_mix_f32 v113, v37, v49, v61 op_sel_hi:[1,0,0]\n\t"
    "v_fma_mix_f32 v114, v38, v50, v62 op_sel_hi:[1,0,0]\n\t"
    "v_fma_mix_f32 v115, v39, v51, v63 op_sel_hi:[1,0,0]\n\t"
    "ds_write_b64 v151, v[130:131] offset:2064\n\t"
    "v_mul_f32 v112, v112, v88\n\t"
    "v_mul_f32 v113, v113, v88\n\t"
    "v_mul_f32 v114, v114, v88\n\t"
    "v_mul_f32 v115, v115, v88\n\t"
    "ds_write_b64 v151, v[132:133] offset:4128\n\t"
    "v_cvt_pk_bf16_f32 v116, v112, v113\n\t"
    "v_cvt_pk_bf16_f32 v117, v114, v115\n\t"
    "ds_write_b64 v151, v[134:135] offset:6192\n\t"
    "ds_write_b64 v152, v[116:117] offset:0\n\t"
    "buffer_load_dwordx4 v[36:39], v140, s[20:23], s49 offen\n\t"
    "buffer_load_dwordx4 v148, s[24:27], s58 offen offset:2048  lds\n\t"
    "buffer_load_dwordx4 v149, s[24:27], s58 offen offset:3072  lds\n\t"
    "s_waitcnt vmcnt(17) lgkmcnt(0)\n\t"
    "s_barrier\n\t"
    "s_waitcnt vmcnt(16)\n\t"
    "v_fma_mix_f32 v112, v4, v40, v52 op_sel_hi:[1,0,0]\n\t"
    "v_fma_mix_f32 v113, v5, v41, v53 op_sel_hi:[1,0,0]\n\t"
    "v_fma_mix_f32 v114, v6, v42, v54 op_sel_hi:[1,0,0]\n\t"
    "v_fma_mix_f32 v115, v7, v43, v55 op_sel_hi:[1,0,0]\n\t"
    "buffer_load_dwordx4 v[4:7], v136, s[20:23], s50 offen offset:0\n\t"
    "s_add_u32 m0, s74, 0\n\t"
    "v_mul_f32 v112, v112, v89\n\t"
    "v_mul_f32 v113, v113, v89\n\t"
    "v_mul_f32 v114, v114, v89\n\t"
    "v_mul_f32 v115, v115, v89\n\t"
    "buffer_load_dwordx4 v142, s[24:27], s59 offen offset:0  lds\n\t"
    "s_waitcnt vmcnt(16)\n\t"
    "v_fma_mix_f32 v116, v8, v40, v52 op_sel_hi:[1,0,0]\n\t"
    "v_fma_mix_f32 v117, v9, v41, v53 op_sel_hi:[1,0,0]\n\t"
    "v_fma_mix_f32 v118, v10, v42, v54 op_sel_hi:[1,0,0]\n\t"
    "v_fma_mix_f32 v119, v11, v43, v55 op_sel_hi:[1,0,0]\n\t"
    "buffer_load_dwordx4 v[8:11], v137, s[20:23], s50 offen offset:0\n\t"
    "v_mul_f32 v116, v116, v90\n\t"
    "v_mul_f32 v117, v117, v90\n\t"
    "v_mul_f32 v118, v118, v90\n\t"
    "v_mul_f32 v119, v119, v90\n\t"
    "buffer_load_dwordx4 v143, s[24:27], s59 offen offset:1024  lds\n\t"
    "v_cvt_pk_bf16_f32 v120, v112, v116\n\t"
    "v_cvt_pk_bf16_f32 v122, v113, v117\n\t"
    "v_cvt_pk_bf16_f32 v124, v114, v118\n\t"
    "v_cvt_pk_bf16_f32 v126, v115, v119\n\t"
    "s_waitcnt vmcnt(16)\n\t"
    "v_fma_mix_f32 v112, v12, v40, v52 op_sel_hi:[1,0,0]\n\t"
    "v_fma_mix_f32 v113, v13, v41, v53 op_sel_hi:[1,0,0]\n\t"
    "v_fma_mix_f32 v114, v14, v42, v54 op_sel_hi:[1,0,0]\n\t"
    "v_fma_mix_f32 v115, v15, v43, v55 op_sel_hi:[1,0,0]\n\t"
    "buffer_load_dwordx4 v[12:15], v138, s[20:23], s50 offen offset:0\n\t"
    "v_mul_f32 v112, v112, v91\n\t"
    "v_mul_f32 v113, v113, v91\n\t"
    "v_mul_f32 v114, v114, v91\n\t"
    "v_mul_f32 v115, v115, v91\n\t"
    "buffer_load_dwordx4 v144, s[24:27], s59 offen offset:2048  lds\n\t"
    "s_waitcnt vmcnt(16)\n\t"
    "v_fma_mix_f32 v116, v16, v40, v52 op_sel_hi:[1,0,0]\n\t"
    "v_fma_mix_f32 v117, v17, v41, v53 op_sel_hi:[1,0,0]\n\t"
    "v_fma_mix_f32 v118, v18, v42, v54 op_sel_hi:[1,0,0]\n\t"
    "v_fma_mix_f32 v119, v19, v43, v55 op_sel_hi:[1,0,0]\n\t"
    "buffer_load_dwordx4 v[16:19], v139, s[20:23], s50 offen offset:0\n\t"
    "v_mul_f32 v116, v116, v92\n\t"
    "v_mul_f32 v117, v117, v92\n\t"
    "v_mul_f32 v118, v118, v92\n\t"
    "v_mul_f32 v119, v119, v92\n\t"
    "buffer_load_dwordx4 v145, s[24:27], s59 offen offset:3072  lds\n\t"
    "v_cvt_pk_bf16_f32 v121, v112, v116\n\t"
    "v_cvt_pk_bf16_f32 v123, v113, v117\n\t"
    "v_cvt_pk_bf16_f32 v125, v114, v118\n\t"
    "v_cvt_pk_bf16_f32 v127, v115, v119\n\t"
    "s_waitcnt vmcnt(16)\n\t"
    "v_fma_mix_f32 v112, v20, v44, v56 op_sel_hi:[1,0,0]\n\t"
    "v_fma_mix_f32 v113, v21, v45, v57 op_sel_hi:[1,0,0]\n\t"
    "v_fma_mix_f32 v114, v22, v46, v58 op_sel_hi:[1,0,0]\n\t"
    "v_fma_mix_f32 v115, v23, v47, v59 op_sel_hi:[1,0,0]\n\t"
    "buffer_load_dwordx4 v[20:23], v136, s[20:23], s50 offen offset:128\n\t"
    "s_add_u32 m0, s74, 4096\n\t"
    "v_mul_f32 v112, v112, v89\n\t"
    "v_mul_f32 v113, v113, v89\n\t"
    "v_mul_f32 v114, v114, v89\n\t"
    "v_mul_f32 v115, v115, v89\n\t"
    "buffer_load_dwordx4 v146, s[24:27], s59 offen offset:0  lds\n\t"
    "ds_write_b64 v150, v[120:121] offset:18560\n\t"
    "s_waitcnt vmcnt(16)\n\t"
    "v_fma_mix_f32 v116, v24, v44, v56 op_sel_hi:[1,0,0]\n\t"
    "v_fma_mix_f32 v117, v25, v45, v57 op_sel_hi:[1,0,0]\n\t"
    "v_fma_mix_f32 v118, v26, v46, v58 op_sel_hi:[1,0,0]\n\t"
    "v_fma_mix_f32 v119, v27, v47, v59 op_sel_hi:[1,0,0]\n\t"
    "buffer_load_dwordx4 v[24:27], v137, s[20:23], s50 offen offset:128\n\t"
    "v_mul_f32 v116, v116, v90\n\t"
    "v_mul_f32 v117, v117, v90\n\t"
    "v_mul_f32 v118, v118, v90\n\t"
    "v_mul_f32 v119, v119, v90\n\t"
    "buffer_load_dwordx4 v147, s[24:27], s59 offen offset:1024  lds\n\t"
    "ds_write_b64 v150, v[122:123] offset:20624\n\t"
    "v_cvt_pk_bf16_f32 v128, v112, v116\n\t"
    "v_cvt_pk_bf16_f32 v130, v113, v117\n\t"
    "v_cvt_pk_bf16_f32 v132, v114, v118\n\t"
    "v_cvt_pk_bf16_f32 v134, v115, v119\n\t"
    "s_waitcnt vmcnt(16)\n\t"
    "v_fma_mix_f32 v112, v28, v44, v56 op_sel_hi:[1,0,0]\n\t"
    "v_fma_mix_f32 v113, v29, v45, v57 op_sel_hi:[1,0,0]\n\t"
    "v_fma_mix_f32 v114, v30, v46, v58 op_sel_hi:[1,0,0]\n\t"
    "v_fma_mix_f32 v115, v31, v47, v59 op_sel_hi:[1,0,0]\n\t"
    "buffer_load_dwordx4 v[28:31], v138, s[20:23], s50 offen offset:128\n\t"
    "v_mul_f32 v112, v112, v91\n\t"
    "v_mul_f32 v113, v113, v91\n\t"
    "v_mul_f32 v114, v114, v91\n\t"
    "v_mul_f32 v115, v115, v91\n\t"
    "ds_write_b64 v150, v[124:125] offset:22688\n\t"
    "s_waitcnt vmcnt(16)\n\t"
    "v_fma_mix_f32 v116, v32, v44, v56 op_sel_hi:[1,0,0]\n\t"
    "v_fma_mix_f32 v117, v33, v45, v57 op_sel_hi:[1,0,0]\n\t"
    "v_fma_mix_f32 v118, v34, v46, v58 op_sel_hi:[1,0,0]\n\t"
    "v_fma_mix_f32 v119, v35, v47, v59 op_sel_hi:[1,0,0]\n\t"
    "buffer_load_dwordx4 v[32:35], v139, s[20:23], s50 offen offset:128\n\t"
    "v_mul_f32 v116, v116, v92\n\t"
    "v_mul_f32 v117, v117, v92\n\t"
    "v_mul_f32 v118, v118, v92\n\t"
    "v_mul_f32 v119, v119, v92\n\t"
    "ds_write_b64 v150, v[126:127] offset:24752\n\t"
    "v_cvt_pk_bf16_f32 v129, v112, v116\n\t"
    "v_cvt_pk_bf16_f32 v131, v113, v117\n\t"
    "v_cvt_pk_bf16_f32 v133, v114, v118\n\t"
    "v_cvt_pk_bf16_f32 v135, v115, v119\n\t"
    "s_waitcnt vmcnt(16)\n\t"
    "ds_write_b64 v151, v[128:129] offset:18560\n\t"
    "v_fma_mix_f32 v112, v36, v48, v60 op_sel_hi:[1,0,0]\n\t"
    "v_fma_mix_f32 v113, v37, v49, v61 op_sel_hi:[1,0,0]\n\t"
    "v_fma_mix_f32 v114, v38, v50, v62 op_sel_hi:[1,0,0]\n\t"
    "v_fma_mix_f32 v115, v39, v51, v63 op_sel_hi:[1,0,0]\n\t"
    "ds_write_b64 v151, v[130:131] offset:20624\n\t"
    "v_mul_f32 v112, v112, v93\n\t"
    "v_mul_f32 v113, v113, v93\n\t"
    "v_mul_f32 v114, v114, v93\n\t"
    "v_mul_f32 v115, v115, v93\n\t"
    "ds_write_b64 v151, v[132:133] offset:22688\n\t"
    "v_cvt_pk_bf16_f32 v116, v112, v113\n\t"
    "v_cvt_pk_bf16_f32 v117, v114, v115\n\t"
    "ds_write_b64 v151, v[134:135] offset:24752\n\t"
    "ds_write_b64 v152, v[116:117] offset:18560\n\t"
    "buffer_load_dwordx4 v[36:39], v140, s[20:23], s50 offen\n\t"
    "buffer_load_dwordx4 v148, s[24:27], s59 offen offset:2048  lds\n\t"
    "buffer_load_dwordx4 v149, s[24:27], s59 offen offset:3072  lds\n\t"
    "s_waitcnt vmcnt(17) lgkmcnt(0)\n\t"
    "s_barrier\n\t"
    "s_waitcnt vmcnt(16)\n\t"
    "v_fma_mix_f32 v112, v4, v40, v52 op_sel_hi:[1,0,0]\n\t"
    "v_fma_mix_f32 v113, v5, v41, v53 op_sel_hi:[1,0,0]\n\t"
    "v_fma_mix_f32 v114, v6, v42, v54 op_sel_hi:[1,0,0]\n\t"
    "v_fma_mix_f32 v115, v7, v43, v55 op_sel_hi:[1,0,0]\n\t"
    "buffer_load_dwordx4 v[4:7], v136, s[20:23], s51 offen offset:0\n\t"
    "s_add_u32 m0, s74, 32768\n\t"
    "v_mul_f32 v112, v112, v94\n\t"
    "v_mul_f32 v113, v113, v94\n\t"
    "v_mul_f32 v114, v114, v94\n\t"
    "v_mul_f32 v115, v115, v94\n\t"
    "buffer_load_dwordx4 v142, s[24:27], s60 offen offset:0  lds\n\t"
    "s_waitcnt vmcnt(16)\n\t"
    "v_fma_mix_f32 v116, v8, v40, v52 op_sel_hi:[1,0,0]\n\t"
    "v_fma_mix_f32 v117, v9, v41, v53 op_sel_hi:[1,0,0]\n\t"
    "v_fma_mix_f32 v118, v10, v42, v54 op_sel_hi:[1,0,0]\n\t"
    "v_fma_mix_f32 v119, v11, v43, v55 op_sel_hi:[1,0,0]\n\t"
    "buffer_load_dwordx4 v[8:11], v137, s[20:23], s51 offen offset:0\n\t"
    "v_mul_f32 v116, v116, v95\n\t"
    "v_mul_f32 v117, v117, v95\n\t"
    "v_mul_f32 v118, v118, v95\n\t"
    "v_mul_f32 v119, v119, v95\n\t"
    "buffer_load_dwordx4 v143, s[24:27], s60 offen offset:1024  lds\n\t"
    "v_cvt_pk_bf16_f32 v120, v112, v116\n\t"
    "v_cvt_pk_bf16_f32 v122, v113, v117\n\t"
    "v_cvt_pk_bf16_f32 v124, v114, v118\n\t"
    "v_cvt_pk_bf16_f32 v126, v115, v119\n\t"
    "s_waitcnt vmcnt(16)\n\t"
    "v_fma_mix_f32 v112, v12, v40, v52 op_sel_hi:[1,0,0]\n\t"
    "v_fma_mix_f32 v113, v13, v41, v53 op_sel_hi:[1,0,0]\n\t"
    "v_fma_mix_f32 v114, v14, v42, v54 op_sel_hi:[1,0,0]\n\t"
    "v_fma_mix_f32 v115, v15, v43, v55 op_sel_hi:[1,0,0]\n\t"
    "buffer_load_dwordx4 v[12:15], v138, s[20:23], s51 offen offset:0\n\t"
    "v_mul_f32 v112, v112, v96\n\t"
    "v_mul_f32 v113, v113, v96\n\t"
    "v_mul_f32 v114, v114, v96\n\t"
    "v_mul_f32 v115, v115, v96\n\t"
    "buffer_load_dwordx4 v144, s[24:27], s60 offen offset:2048  lds\n\t"
    "s_waitcnt vmcnt(16)\n\t"
    "v_fma_mix_f32 v116, v16, v40, v52 op_sel_hi:[1,0,0]\n\t"
    "v_fma_mix_f32 v117, v17, v41, v53 op_sel_hi:[1,0,0]\n\t"
    "v_fma_mix_f32 v118, v18, v42, v54 op_sel_hi:[1,0,0]\n\t"
    "v_fma_mix_f32 v119, v19, v43, v55 op_sel_hi:[1,0,0]\n\t"
    "buffer_load_dwordx4 v[16:19], v139, s[20:23], s51 offen offset:0\n\t"
    "v_mul_f32 v116, v116, v97\n\t"
    "v_mul_f32 v117, v117, v97\n\t"
    "v_mul_f32 v118, v118, v97\n\t"
    "v_mul_f32 v119, v119, v97\n\t"
    "buffer_load_dwordx4 v145, s[24:27], s60 offen offset:3072  lds\n\t"
    "v_cvt_pk_bf16_f32 v121, v112, v116\n\t"
    "v_cvt_pk_bf16_f32 v123, v113, v117\n\t"
    "v_cvt_pk_bf16_f32 v125, v114, v118\n\t"
    "v_cvt_pk_bf16_f32 v127, v115, v119\n\t"
    "s_waitcnt vmcnt(16)\n\t"
    "v_fma_mix_f32 v112, v20, v44, v56 op_sel_hi:[1,0,0]\n\t"
    "v_fma_mix_f32 v113, v21, v45, v57 op_sel_hi:[1,0,0]\n\t"
    "v_fma_mix_f32 v114, v22, v46, v58 op_sel_hi:[1,0,0]\n\t"
    "v_fma_mix_f32 v115, v23, v47, v59 op_sel_hi:[1,0,0]\n\t"
    "buffer_load_dwordx4 v[20:23], v136, s[20:23], s51 offen offset:128\n\t"
    "s_add_u32 m0, s74, 36864\n\t"
    "v_mul_f32 v112, v112, v94\n\t"
    "v_mul_f32 v113, v113, v94\n\t"
    "v_mul_f32 v114, v114, v94\n\t"
    "v_mul_f32 v115, v115, v94\n\t"
    "buffer_load_dwordx4 v146, s[24:27], s60 offen offset:0  lds\n\t"
    "ds_write_b64 v150, v[120:121] offset:0\n\t"
    "s_waitcnt vmcnt(16)\n\t"
    "v_fma_mix_f32 v116, v24, v44, v56 op_sel_hi:[1,0,0]\n\t"
    "v_fma_mix_f32 v117, v25, v45, v57 op_sel_hi:[1,0,0]\n\t"
    "v_fma_mix_f32 v118, v26, v46, v58 op_sel_hi:[1,0,0]\n\t"
    "v_fma_mix_f32 v119, v27, v47, v59 op_sel_hi:[1,0,0]\n\t"
    "buffer_load_dwordx4 v[24:27], v137, s[20:23], s51 offen offset:128\n\t"
    "v_mul_f32 v116, v116, v95\n\t"
    "v_mul_f32 v117, v117, v95\n\t"
    "v_mul_f32 v118, v118, v95\n\t"
    "v_mul_f32 v119, v119, v95\n\t"
    "buffer_load_dwordx4 v147, s[24:27], s60 offen offset:1024  lds\n\t"
    "ds_write_b64 v150, v[122:123] offset:2064\n\t"
    "v_cvt_pk_bf16_f32 v128, v112, v116\n\t"
    "v_cvt_pk_bf16_f32 v130, v113, v117\n\t"
    "v_cvt_pk_bf16_f32 v132, v114, v118\n\t"
    "v_cvt_pk_bf16_f32 v134, v115, v119\n\t"
    "s_waitcnt vmcnt(16)\n\t"
    "v_fma_mix_f32 v112, v28, v44, v56 op_sel_hi:[1,0,0]\n\t"
    "v_fma_mix_f32 v113, v29, v45, v57 op_sel_hi:[1,0,0]\n\t"
    "v_fma_mix_f32 v114, v30, v46, v58 op_sel_hi:[1,0,0]\n\t"
    "v_fma_mix_f32 v115, v31, v47, v59 op_sel_hi:[1,0,0]\n\t"
    "buffer_load_dwordx4 v[28:31], v138, s[20:23], s51 offen offset:128\n\t"
    "v_mul_f32 v112, v112, v96\n\t"
    "v_mul_f32 v113, v113, v96\n\t"
    "v_mul_f32 v114, v114, v96\n\t"
    "v_mul_f32 v115, v115, v96\n\t"
    "ds_write_b64 v150, v[124:125] offset:4128\n\t"
    "s_waitcnt vmcnt(16)\n\t"
    "v_fma_mix_f32 v116, v32, v44, v56 op_sel_hi:[1,0,0]\n\t"
    "v_fma_mix_f32 v117, v33, v45, v57 op_sel_hi:[1,0,0]\n\t"
    "v_fma_mix_f32 v118, v34, v46, v58 op_sel_hi:[1,0,0]\n\t"
    "v_fma_mix_f32 v119, v35, v47, v59 op_sel_hi:[1,0,0]\n\t"
    "buffer_load_dwordx4 v[32:35], v139, s[20:23], s51 offen offset:128\n\t"
    "v_mul_f32 v116, v116, v97\n\t"
    "v_mul_f32 v117, v117, v97\n\t"
    "v_mul_f32 v118, v118, v97\n\t"
    "v_mul_f32 v119, v119, v97\n\t"
    "ds_write_b64 v150, v[126:127] offset:6192\n\t"
    "v_cvt_pk_bf16_f32 v129, v112, v116\n\t"
    "v_cvt_pk_bf16_f32 v131, v113, v117\n\t"
    "v_cvt_pk_bf16_f32 v133, v114, v118\n\t"
    "v_cvt_pk_bf16_f32 v135, v115, v119\n\t"
    "s_waitcnt vmcnt(16)\n\t"
    "ds_write_b64 v151, v[128:129] offset:0\n\t"
    "v_fma_mix_f32 v112, v36, v48, v60 op_sel_hi:[1,0,0]\n\t"
    "v_fma_mix_f32 v113, v37, v49, v61 op_sel_hi:[1,0,0]\n\t"
    "v_fma_mix_f32 v114, v38, v50, v62 op_sel_hi:[1,0,0]\n\t"
    "v_fma_mix_f32 v115, v39, v51, v63 op_sel_hi:[1,0,0]\n\t"
    "ds_write_b64 v151, v[130:131] offset:2064\n\t"
    "v_mul_f32 v112, v112, v98\n\t"
    "v_mul_f32 v113, v113, v98\n\t"
    "v_mul_f32 v114, v114, v98\n\t"
    "v_mul_f32 v115, v115, v98\n\t"
    "ds_write_b64 v151, v[132:133] offset:4128\n\t"
    "v_cvt_pk_bf16_f32 v116, v112, v113\n\t"
    "v_cvt_pk_bf16_f32 v117, v114, v115\n\t"
    "ds_write_b64 v151, v[134:135] offset:6192\n\t"
    "ds_write_b64 v152, v[116:117] offset:0\n\t"
    "buffer_load_dwordx4 v[36:39], v140, s[20:23], s51 offen\n\t"
    "buffer_load_dwordx4 v148, s[24:27], s60 offen offset:2048  lds\n\t"
    "buffer_load_dwordx4 v149, s[24:27], s60 offen offset:3072  lds\n\t"
    "s_waitcnt vmcnt(17) lgkmcnt(0)\n\t"
    "s_barrier\n\t"
    "s_waitcnt vmcnt(16)\n\t"
    "v_fma_mix_f32 v112, v4, v40, v52 op_sel_hi:[1,0,0]\n\t"
    "v_fma_mix_f32 v113, v5, v41, v53 op_sel_hi:[1,0,0]\n\t"
    "v_fma_mix_f32 v114, v6, v42, v54 op_sel_hi:[1,0,0]\n\t"
    "v_fma_mix_f32 v115, v7, v43, v55 op_sel_hi:[1,0,0]\n\t"
    "buffer_load_dwordx4 v[4:7], v136, s[20:23], s52 offen offset:0\n\t"
    "s_add_u32 m0, s74, 65536\n\t"
    "v_mul_f32 v112, v112, v99\n\t"
    "v_mul_f32 v113, v113, v99\n\t"
    "v_mul_f32 v114, v114, v99\n\t"
    "v_mul_f32 v115, v115, v99\n\t"
    "buffer_load_dwordx4 v142, s[24:27], s61 offen offset:0  lds\n\t"
    "s_waitcnt vmcnt(16)\n\t"
    "v_fma_mix_f32 v116, v8, v40, v52 op_sel_hi:[1,0,0]\n\t"
    "v_fma_mix_f32 v117, v9, v41, v53 op_sel_hi:[1,0,0]\n\t"
    "v_fma_mix_f32 v118, v10, v42, v54 op_sel_hi:[1,0,0]\n\t"
    "v_fma_mix_f32 v119, v11, v43, v55 op_sel_hi:[1,0,0]\n\t"
    "buffer_load_dwordx4 v[8:11], v137, s[20:23], s52 offen offset:0\n\t"
    "v_mul_f32 v116, v116, v100\n\t"
    "v_mul_f32 v117, v117, v100\n\t"
    "v_mul_f32 v118, v118, v100\n\t"
    "v_mul_f32 v119, v119, v100\n\t"
    "buffer_load_dwordx4 v143, s[24:27], s61 offen offset:1024  lds\n\t"
    "v_cvt_pk_bf16_f32 v120, v112, v116\n\t"
    "v_cvt_pk_bf16_f32 v122, v113, v117\n\t"
    "v_cvt_pk_bf16_f32 v124, v114, v118\n\t"
    "v_cvt_pk_bf16_f32 v126, v115, v119\n\t"
    "s_waitcnt vmcnt(16)\n\t"
    "v_fma_mix_f32 v112, v12, v40, v52 op_sel_hi:[1,0,0]\n\t"
    "v_fma_mix_f32 v113, v13, v41, v53 op_sel_hi:[1,0,0]\n\t"
    "v_fma_mix_f32 v114, v14, v42, v54 op_sel_hi:[1,0,0]\n\t"
    "v_fma_mix_f32 v115, v15, v43, v55 op_sel_hi:[1,0,0]\n\t"
    "buffer_load_dwordx4 v[12:15], v138, s[20:23], s52 offen offset:0\n\t"
    "v_mul_f32 v112, v112, v101\n\t"
    "v_mul_f32 v113, v113, v101\n\t"
    "v_mul_f32 v114, v114, v101\n\t"
    "v_mul_f32 v115, v115, v101\n\t"
    "buffer_load_dwordx4 v144, s[24:27], s61 offen offset:2048  lds\n\t"
    "s_waitcnt vmcnt(16)\n\t"
    "v_fma_mix_f32 v116, v16, v40, v52 op_sel_hi:[1,0,0]\n\t"
    "v_fma_mix_f32 v117, v17, v41, v53 op_sel_hi:[1,0,0]\n\t"
    "v_fma_mix_f32 v118, v18, v42, v54 op_sel_hi:[1,0,0]\n\t"
    "v_fma_mix_f32 v119, v19, v43, v55 op_sel_hi:[1,0,0]\n\t"
    "buffer_load_dwordx4 v[16:19], v139, s[20:23], s52 offen offset:0\n\t"
    "v_mul_f32 v116, v116, v102\n\t"
    "v_mul_f32 v117, v117, v102\n\t"
    "v_mul_f32 v118, v118, v102\n\t"
    "v_mul_f32 v119, v119, v102\n\t"
    "buffer_load_dwordx4 v145, s[24:27], s61 offen offset:3072  lds\n\t"
    "v_cvt_pk_bf16_f32 v121, v112, v116\n\t"
    "v_cvt_pk_bf16_f32 v123, v113, v117\n\t"
    "v_cvt_pk_bf16_f32 v125, v114, v118\n\t"
    "v_cvt_pk_bf16_f32 v127, v115, v119\n\t"
    "s_waitcnt vmcnt(16)\n\t"
    "v_fma_mix_f32 v112, v20, v44, v56 op_sel_hi:[1,0,0]\n\t"
    "v_fma_mix_f32 v113, v21, v45, v57 op_sel_hi:[1,0,0]\n\t"
    "v_fma_mix_f32 v114, v22, v46, v58 op_sel_hi:[1,0,0]\n\t"
    "v_fma_mix_f32 v115, v23, v47, v59 op_sel_hi:[1,0,0]\n\t"
    "buffer_load_dwordx4 v[20:23], v136, s[20:23], s52 offen offset:128\n\t"
    "s_add_u32 m0, s74, 69632\n\t"
    "v_mul_f32 v112, v112, v99\n\t"
    "v_mul_f32 v113, v113, v99\n\t"
    "v_mul_f32 v114, v114, v99\n\t"
    "v_mul_f32 v115, v115, v99\n\t"
    "buffer_load_dwordx4 v146, s[24:27], s61 offen offset:0  lds\n\t"
    "ds_write_b64 v150, v[120:121] offset:18560\n\t"
    "s_waitcnt vmcnt(16)\n\t"
    "v_fma_mix_f32 v116, v24, v44, v56 op_sel_hi:[1,0,0]\n\t"
    "v_fma_mix_f32 v117, v25, v45, v57 op_sel_hi:[1,0,0]\n\t"
    "v_fma_mix_f32 v118, v26, v46, v58 op_sel_hi:[1,0,0]\n\t"
    "v_fma_mix_f32 v119, v27, v47, v59 op_sel_hi:[1,0,0]\n\t"
    "buffer_load_dwordx4 v[24:27], v137, s[20:23], s52 offen offset:128\n\t"
    "v_mul_f32 v116, v116, v100\n\t"
    "v_mul_f32 v117, v117, v100\n\t"
    "v_mul_f32 v118, v118, v100\n\t"
    "v_mul_f32 v119, v119, v100\n\t"
    "buffer_load_dwordx4 v147, s[24:27], s61 offen offset:1024  lds\n\t"
    "ds_write_b64 v150, v[122:123] offset:20624\n\t"
    "v_cvt_pk_bf16_f32 v128, v112, v116\n\t"
    "v_cvt_pk_bf16_f32 v130, v113, v117\n\t"
    "v_cvt_pk_bf16_f32 v132, v114, v118\n\t"
    "v_cvt_pk_bf16_f32 v134, v115, v119\n\t"
    "s_waitcnt vmcnt(16)\n\t"
    "v_fma_mix_f32 v112, v28, v44, v56 op_sel_hi:[1,0,0]\n\t"
    "v_fma_mix_f32 v113, v29, v45, v57 op_sel_hi:[1,0,0]\n\t"
    "v_fma_mix_f32 v114, v30, v46, v58 op_sel_hi:[1,0,0]\n\t"
    "v_fma_mix_f32 v115, v31, v47, v59 op_sel_hi:[1,0,0]\n\t"
    "buffer_load_dwordx4 v[28:31], v138, s[20:23], s52 offen offset:128\n\t"
    "v_mul_f32 v112, v112, v101\n\t"
    "v_mul_f32 v113, v113, v101\n\t"
    "v_mul_f32 v114, v114, v101\n\t"
    "v_mul_f32 v115, v115, v101\n\t"
    "ds_write_b64 v150, v[124:125] offset:22688\n\t"
    "s_waitcnt vmcnt(16)\n\t"
    "v_fma_mix_f32 v116, v32, v44, v56 op_sel_hi:[1,0,0]\n\t"
    "v_fma_mix_f32 v117, v33, v45, v57 op_sel_hi:[1,0,0]\n\t"
    "v_fma_mix_f32 v118, v34, v46, v58 op_sel_hi:[1,0,0]\n\t"
    "v_fma_mix_f32 v119, v35, v47, v59 op_sel_hi:[1,0,0]\n\t"
    "buffer_load_dwordx4 v[32:35], v139, s[20:23], s52 offen offset:128\n\t"
    "v_mul_f32 v116, v116, v102\n\t"
    "v_mul_f32 v117, v117, v102\n\t"
    "v_mul_f32 v118, v118, v102\n\t"
    "v_mul_f32 v119, v119, v102\n\t"
    "ds_write_b64 v150, v[126:127] offset:24752\n\t"
    "v_cvt_pk_bf16_f32 v129, v112, v116\n\t"
    "v_cvt_pk_bf16_f32 v131, v113, v117\n\t"
    "v_cvt_pk_bf16_f32 v133, v114, v118\n\t"
    "v_cvt_pk_bf16_f32 v135, v115, v119\n\t"
    "s_waitcnt vmcnt(16)\n\t"
    "ds_write_b64 v151, v[128:129] offset:18560\n\t"
    "v_fma_mix_f32 v112, v36, v48, v60 op_sel_hi:[1,0,0]\n\t"
    "v_fma_mix_f32 v113, v37, v49, v61 op_sel_hi:[1,0,0]\n\t"
    "v_fma_mix_f32 v114, v38, v50, v62 op_sel_hi:[1,0,0]\n\t"
    "v_fma_mix_f32 v115, v39, v51, v63 op_sel_hi:[1,0,0]\n\t"
    "ds_write_b64 v151, v[130:131] offset:20624\n\t"
    "v_mul_f32 v112, v112, v103\n\t"
    "v_mul_f32 v113, v113, v103\n\t"
    "v_mul_f32 v114, v114, v103\n\t"
    "v_mul_f32 v115, v115, v103\n\t"
    "ds_write_b64 v151, v[132:133] offset:22688\n\t"
    "v_cvt_pk_bf16_f32 v116, v112, v113\n\t"
    "v_cvt_pk_bf16_f32 v117, v114, v115\n\t"
    "ds_write_b64 v151, v[134:135] offset:24752\n\t"
    "ds_write_b64 v152, v[116:117] offset:18560\n\t"
    "buffer_load_dwordx4 v[36:39], v140, s[20:23], s52 offen\n\t"
    "buffer_load_dwordx4 v148, s[24:27], s61 offen offset:2048  lds\n\t"
    "buffer_load_dwordx4 v149, s[24:27], s61 offen offset:3072  lds\n\t"
    "s_waitcnt vmcnt(17) lgkmcnt(0)\n\t"
    "s_barrier\n\t"
    "s_waitcnt vmcnt(16)\n\t"
    "v_fma_mix_f32 v112, v4, v40, v52 op_sel_hi:[1,0,0]\n\t"
    "v_fma_mix_f32 v113, v5, v41, v53 op_sel_hi:[1,0,0]\n\t"
    "v_fma_mix_f32 v114, v6, v42, v54 op_sel_hi:[1,0,0]\n\t"
    "v_fma_mix_f32 v115, v7, v43, v55 op_sel_hi:[1,0,0]\n\t"
    "v_mul_f32 v112, v112, v104\n\t"
    "v_mul_f32 v113, v113, v104\n\t"
    "v_mul_f32 v114, v114, v104\n\t"
    "v_mul_f32 v115, v115, v104\n\t"
    "s_waitcnt vmcnt(14)\n\t"
    "v_fma_mix_f32 v116, v8, v40, v52 op_sel_hi:[1,0,0]\n\t"
    "v_fma_mix_f32 v117, v9, v41, v53 op_sel_hi:[1,0,0]\n\t"
    "v_fma_mix_f32 v118, v10, v42, v54 op_sel_hi:[1,0,0]\n\t"
    "v_fma_mix_f32 v119, v11, v43, v55 op_sel_hi:[1,0,0]\n\t"
    "v_mul_f32 v116, v116, v105\n\t"
    "v_mul_f32 v117, v117, v105\n\t"
    "v_mul_f32 v118, v118, v105\n\t"
    "v_mul_f32 v119, v119, v105\n\t"
    "v_cvt_pk_bf16_f32 v120, v112, v116\n\t"
    "v_cvt_pk_bf16_f32 v122, v113, v117\n\t"
    "v_cvt_pk_bf16_f32 v124, v114, v118\n\t"
    "v_cvt_pk_bf16_f32 v126, v115, v119\n\t"
    "s_waitcnt vmcnt(12)\n\t"
    "v_fma_mix_f32 v112, v12, v40, v52 op_sel_hi:[1,0,0]\n\t"
    "v_fma_mix_f32 v113, v13, v41, v53 op_sel_hi:[1,0,0]\n\t"
    "v_fma_mix_f32 v114, v14, v42, v54 op_sel_hi:[1,0,0]\n\t"
    "v_fma_mix_f32 v115, v15, v43, v55 op_sel_hi:[1,0,0]\n\t"
    "v_mul_f32 v112, v112, v106\n\t"
    "v_mul_f32 v113, v113, v106\n\t"
    "v_mul_f32 v114, v114, v106\n\t"
    "v_mul_f32 v115, v115, v106\n\t"
    "s_waitcnt vmcnt(10)\n\t"
    "v_fma_mix_f32 v116, v16, v40, v52 op_sel_hi:[1,0,0]\n\t"
    "v_fma_mix_f32 v117, v17, v41, v53 op_sel_hi:[1,0,0]\n\t"
    "v_fma_mix_f32 v118, v18, v42, v54 op_sel_hi:[1,0,0]\n\t"
    "v_fma_mix_f32 v119, v19, v43, v55 op_sel_hi:[1,0,0]\n\t"
    "v_mul_f32 v116, v116, v107\n\t"
    "v_mul_f32 v117, v117, v107\n\t"
    "v_mul_f32 v118, v118, v107\n\t"
    "v_mul_f32 v119, v119, v107\n\t"
    "v_cvt_pk_bf16_f32 v121, v112, v116\n\t"
    "v_cvt_pk_bf16_f32 v123, v113, v117\n\t"
    "v_cvt_pk_bf16_f32 v125, v114, v118\n\t"
    "v_cvt_pk_bf16_f32 v127, v115, v119\n\t"
    "s_waitcnt vmcnt(8)\n\t"
    "v_fma_mix_f32 v112, v20, v44, v56 op_sel_hi:[1,0,0]\n\t"
    "v_fma_mix_f32 v113, v21, v45, v57 op_sel_hi:[1,0,0]\n\t"
    "v_fma_mix_f32 v114, v22, v46, v58 op_sel_hi:[1,0,0]\n\t"
    "v_fma_mix_f32 v115, v23, v47, v59 op_sel_hi:[1,0,0]\n\t"
    "v_mul_f32 v112, v112, v104\n\t"
    "v_mul_f32 v113, v113, v104\n\t"
    "v_mul_f32 v114, v114, v104\n\t"
    "v_mul_f32 v115, v115, v104\n\t"
    "ds_write_b64 v150, v[120:121] offset:0\n\t"
    "s_waitcnt vmcnt(6)\n\t"
    "v_fma_mix_f32 v116, v24, v44, v56 op_sel_hi:[1,0,0]\n\t"
    "v_fma_mix_f32 v117, v25, v45, v57 op_sel_hi:[1,0,0]\n\t"
    "v_fma_mix_f32 v118, v26, v46, v58 op_sel_hi:[1,0,0]\n\t"
    "v_fma_mix_f32 v119, v27, v47, v59 op_sel_hi:[1,0,0]\n\t"
    "v_mul_f32 v116, v116, v105\n\t"
    "v_mul_f32 v117, v117, v105\n\t"
    "v_mul_f32 v118, v118, v105\n\t"
    "v_mul_f32 v119, v119, v105\n\t"
    "ds_write_b64 v150, v[122:123] offset:2064\n\t"
    "v_cvt_pk_bf16_f32 v128, v112, v116\n\t"
    "v_cvt_pk_bf16_f32 v130, v113, v117\n\t"
    "v_cvt_pk_bf16_f32 v132, v114, v118\n\t"
    "v_cvt_pk_bf16_f32 v134, v115, v119\n\t"
    "s_waitcnt vmcnt(4)\n\t"
    "v_fma_mix_f32 v112, v28, v44, v56 op_sel_hi:[1,0,0]\n\t"
    "v_fma_mix_f32 v113, v29, v45, v57 op_sel_hi:[1,0,0]\n\t"
    "v_fma_mix_f32 v114, v30, v46, v58 op_sel_hi:[1,0,0]\n\t"
    "v_fma_mix_f32 v115, v31, v47, v59 op_sel_hi:[1,0,0]\n\t"
    "v_mul_f32 v112, v112, v106\n\t"
    "v_mul_f32 v113, v113, v106\n\t"
    "v_mul_f32 v114, v114, v106\n\t"
    "v_mul_f32 v115, v115, v106\n\t"
    "ds_write_b64 v150, v[124:125] offset:4128\n\t"
    "s_waitcnt vmcnt(3)\n\t"
    "v_fma_mix_f32 v116, v32, v44, v56 op_sel_hi:[1,0,0]\n\t"
    "v_fma_mix_f32 v117, v33, v45, v57 op_sel_hi:[1,0,0]\n\t"
    "v_fma_mix_f32 v118, v34, v46, v58 op_sel_hi:[1,0,0]\n\t"
    "v_fma_mix_f32 v119, v35, v47, v59 op_sel_hi:[1,0,0]\n\t"
    "v_mul_f32 v116, v116, v107\n\t"
    "v_mul_f32 v117, v117, v107\n\t"
    "v_mul_f32 v118, v118, v107\n\t"
    "v_mul_f32 v119, v119, v107\n\t"
    "ds_write_b64 v150, v[126:127] offset:6192\n\t"
    "v_cvt_pk_bf16_f32 v129, v112, v116\n\t"
    "v_cvt_pk_bf16_f32 v131, v113, v117\n\t"
    "v_cvt_pk_bf16_f32 v133, v114, v118\n\t"
    "v_cvt_pk_bf16_f32 v135, v115, v119\n\t"
    "s_waitcnt vmcnt(2)\n\t"
    "ds_write_b64 v151, v[128:129] offset:0\n\t"
    "v_fma_mix_f32 v112, v36, v48, v60 op_sel_hi:[1,0,0]\n\t"
    "v_fma_mix_f32 v113, v37, v49, v61 op_sel_hi:[1,0,0]\n\t"
    "v_fma_mix_f32 v114, v38, v50, v62 op_sel_hi:[1,0,0]\n\t"
    "v_fma_mix_f32 v115, v39, v51, v63 op_sel_hi:[1,0,0]\n\t"
    "ds_write_b64 v151, v[130:131] offset:2064\n\t"
    "v_mul_f32 v112, v112, v108\n\t"
    "v_mul_f32 v113, v113, v108\n\t"
    "v_mul_f32 v114, v114, v108\n\t"
    "v_mul_f32 v115, v115, v108\n\t"
    "ds_write_b64 v151, v[132:133] offset:4128\n\t"
    "v_cvt_pk_bf16_f32 v116, v112, v113\n\t"
    "v_cvt_pk_bf16_f32 v117, v114, v115\n\t"
    "ds_write_b64 v151, v[134:135] offset:6192\n\t"
    "ds_write_b64 v152, v[116:117] offset:0\n\t"
    "s_waitcnt vmcnt(0) lgkmcnt(0)\n\t"
    "s_barrier\n\t"
    "s_endpgm\n\t"
    "L_consumer%=:\n\t"
    "v_and_b32 v212, 15, v0\n\t"
    "v_lshrrev_b32 v213, 4, v0\n\t"
    "s_lshl_b32 s72, s64, 6\n\t"
    "s_add_u32 s72, s72, s68\n\t"
    "v_lshl_add_u32 v214, v213, 2, s72\n\t"
    "v_mul_u32_u24 v214, 9216, v214\n\t"
    "s_lshl_b32 s70, s70, 1\n\t"
    "v_lshl_add_u32 v252, v212, 4, s70\n\t"
    "v_add_u32 v252, v252, v214\n\t"
    "s_lshl_b32 s71, s71, 1\n\t"
    "v_lshl_add_u32 v253, v212, 1, s71\n\t"
    "v_add_u32 v253, v253, v214\n\t"
    "s_mov_b32 s44, 0\n\t"
    "s_mov_b32 s45, 9216\n\t"
    "s_mov_b32 s46, 18432\n\t"
    "s_mov_b32 s47, 27648\n\t"
    "s_mov_b32 s48, 147456\n\t"
    "s_mov_b32 s49, 156672\n\t"
    "s_mov_b32 s50, 165888\n\t"
    "s_mov_b32 s51, 175104\n\t"
    "s_mov_b32 s52, 294912\n\t"
    "s_mov_b32 s53, 304128\n\t"
    "s_mov_b32 s54, 313344\n\t"
    "s_mov_b32 s55, 322560\n\t"
    "s_mov_b32 s56, 442368\n\t"
    "s_mov_b32 s57, 451584\n\t"
    "s_mov_b32 s58, 460800\n\t"
    "s_mov_b32 s59, 470016\n\t"
    "v_lshrrev_b32 v214, 1, v212\n\t"
    "v_xor_b32 v214, v214, v213\n\t"
    "v_lshlrev_b32 v214, 4, v214\n\t"
    "v_lshl_add_u32 v214, v212, 7, v214\n\t"
    "s_lshl_b32 s70, s64, 13\n\t"
    "v_add_u32 v248, s70, v214\n\t"
    "v_xor_b32 v249, 64, v248\n\t"
    "v_lshlrev_b32 v250, 3, v0\n\t"
    "v_add_u32 v250, 114816, v250\n\t"
    "v_lshrrev_b32 v214, 2, v212\n\t"
    "v_sub_u32 v214, 0, v214\n\t"
    "v_xor_b32 v214, v214, v212\n\t"
    "v_xor_b32 v214, v214, v213\n\t"
    "v_and_b32 v214, 3, v214\n\t"
    "v_lshlrev_b32 v214, 4, v214\n\t"
    "v_lshl_add_u32 v214, v212, 6, v214\n\t"
    "v_add_u32 v251, 98304, v214\n\t"
    "v_mov_b32 v0, 0\n\t"
    "v_mov_b32 v1, 0\n\t"
    "v_mov_b32 v2, 0\n\t"
    "v_mov_b32 v3, 0\n\t"
    "v_mov_b32 v4, 0\n\t"
    "v_mov_b32 v5, 0\n\t"
    "v_mov_b32 v6, 0\n\t"
    "v_mov_b32 v7, 0\n\t"
    "v_mov_b32 v8, 0\n\t"
    "v_mov_b32 v9, 0\n\t"
    "v_mov_b32 v10, 0\n\t"
    "v_mov_b32 v11, 0\n\t"
    "v_mov_b32 v12, 0\n\t"
    "v_mov_b32 v13, 0\n\t"
    "v_mov_b32 v14, 0\n\t"
    "v_mov_b32 v15, 0\n\t"
    "v_mov_b32 v16, 0\n\t"
    "v_mov_b32 v17, 0\n\t"
    "v_mov_b32 v18, 0\n\t"
    "v_mov_b32 v19, 0\n\t"
    "v_mov_b32 v20, 0\n\t"
    "v_mov_b32 v21, 0\n\t"
    "v_mov_b32 v22, 0\n\t"
    "v_mov_b32 v23, 0\n\t"
    "v_mov_b32 v24, 0\n\t"
    "v_mov_b32 v25, 0\n\t"
    "v_mov_b32 v26, 0\n\t"
    "v_mov_b32 v27, 0\n\t"
    "v_mov_b32 v28, 0\n\t"
    "v_mov_b32 v29, 0\n\t"
    "v_mov_b32 v30, 0\n\t"
    "v_mov_b32 v31, 0\n\t"
    "v_mov_b32 v32, 0\n\t"
    "v_mov_b32 v33, 0\n\t"
    "v_mov_b32 v34, 0\n\t"
    "v_mov_b32 v35, 0\n\t"
    "v_mov_b32 v36, 0\n\t"
    "v_mov_b32 v37, 0\n\t"
    "v_mov_b32 v38, 0\n\t"
    "v_mov_b32 v39, 0\n\t"
    "v_mov_b32 v40, 0\n\t"
    "v_mov_b32 v41, 0\n\t"
    "v_mov_b32 v42, 0\n\t"
    "v_mov_b32 v43, 0\n\t"
    "v_mov_b32 v44, 0\n\t"
    "v_mov_b32 v45, 0\n\t"
    "v_mov_b32 v46, 0\n\t"
    "v_mov_b32 v47, 0\n\t"
    "v_mov_b32 v48, 0\n\t"
    "v_mov_b32 v49, 0\n\t"
    "v_mov_b32 v50, 0\n\t"
    "v_mov_b32 v51, 0\n\t"
    "v_mov_b32 v52, 0\n\t"
    "v_mov_b32 v53, 0\n\t"
    "v_mov_b32 v54, 0\n\t"
    "v_mov_b32 v55, 0\n\t"
    "v_mov_b32 v56, 0\n\t"
    "v_mov_b32 v57, 0\n\t"
    "v_mov_b32 v58, 0\n\t"
    "v_mov_b32 v59, 0\n\t"
    "v_mov_b32 v60, 0\n\t"
    "v_mov_b32 v61, 0\n\t"
    "v_mov_b32 v62, 0\n\t"
    "v_mov_b32 v63, 0\n\t"
    "v_mov_b32 v64, 0\n\t"
    "v_mov_b32 v65, 0\n\t"
    "v_mov_b32 v66, 0\n\t"
    "v_mov_b32 v67, 0\n\t"
    "v_mov_b32 v68, 0\n\t"
    "v_mov_b32 v69, 0\n\t"
    "v_mov_b32 v70, 0\n\t"
    "v_mov_b32 v71, 0\n\t"
    "v_mov_b32 v72, 0\n\t"
    "v_mov_b32 v73, 0\n\t"
    "v_mov_b32 v74, 0\n\t"
    "v_mov_b32 v75, 0\n\t"
    "v_mov_b32 v76, 0\n\t"
    "v_mov_b32 v77, 0\n\t"
    "v_mov_b32 v78, 0\n\t"
    "v_mov_b32 v79, 0\n\t"
    "v_mov_b32 v80, 0\n\t"
    "v_mov_b32 v81, 0\n\t"
    "v_mov_b32 v82, 0\n\t"
    "v_mov_b32 v83, 0\n\t"
    "v_mov_b32 v84, 0\n\t"
    "v_mov_b32 v85, 0\n\t"
    "v_mov_b32 v86, 0\n\t"
    "v_mov_b32 v87, 0\n\t"
    "v_mov_b32 v88, 0\n\t"
    "v_mov_b32 v89, 0\n\t"
    "v_mov_b32 v90, 0\n\t"
    "v_mov_b32 v91, 0\n\t"
    "v_mov_b32 v92, 0\n\t"
    "v_mov_b32 v93, 0\n\t"
    "v_mov_b32 v94, 0\n\t"
    "v_mov_b32 v95, 0\n\t"
    "v_mov_b32 v96, 0\n\t"
    "v_mov_b32 v97, 0\n\t"
    "v_mov_b32 v98, 0\n\t"
    "v_mov_b32 v99, 0\n\t"
    "v_mov_b32 v100, 0\n\t"
    "v_mov_b32 v101, 0\n\t"
    "v_mov_b32 v102, 0\n\t"
    "v_mov_b32 v103, 0\n\t"
    "v_mov_b32 v104, 0\n\t"
    "v_mov_b32 v105, 0\n\t"
    "v_mov_b32 v106, 0\n\t"
    "v_mov_b32 v107, 0\n\t"
    "v_mov_b32 v108, 0\n\t"
    "v_mov_b32 v109, 0\n\t"
    "v_mov_b32 v110, 0\n\t"
    "v_mov_b32 v111, 0\n\t"
    "v_mov_b32 v112, 0\n\t"
    "v_mov_b32 v113, 0\n\t"
    "v_mov_b32 v114, 0\n\t"
    "v_mov_b32 v115, 0\n\t"
    "v_mov_b32 v116, 0\n\t"
    "v_mov_b32 v117, 0\n\t"
    "v_mov_b32 v118, 0\n\t"
    "v_mov_b32 v119, 0\n\t"
    "v_mov_b32 v120, 0\n\t"
    "v_mov_b32 v121, 0\n\t"
    "v_mov_b32 v122, 0\n\t"
    "v_mov_b32 v123, 0\n\t"
    "v_mov_b32 v124, 0\n\t"
    "v_mov_b32 v125, 0\n\t"
    "v_mov_b32 v126, 0\n\t"
    "v_mov_b32 v127, 0\n\t"
    "v_mov_b32 v128, 0\n\t"
    "v_mov_b32 v129, 0\n\t"
    "v_mov_b32 v130, 0\n\t"
    "v_mov_b32 v131, 0\n\t"
    "v_mov_b32 v132, 0\n\t"
    "v_mov_b32 v133, 0\n\t"
    "v_mov_b32 v134, 0\n\t"
    "v_mov_b32 v135, 0\n\t"
    "v_mov_b32 v136, 0\n\t"
    "v_mov_b32 v137, 0\n\t"
    "v_mov_b32 v138, 0\n\t"
    "v_mov_b32 v139, 0\n\t"
    "v_mov_b32 v140, 0\n\t"
    "v_mov_b32 v141, 0\n\t"
    "v_mov_b32 v142, 0\n\t"
    "v_mov_b32 v143, 0\n\t"
    "s_mov_b32 s79, 0\n\t"
    "s_mov_b32 s80, 0\n\t"
    "s_mov_b32 s81, 18560\n\t"
    "s_mov_b32 s72, -65536\n\t"
    "s_mov_b32 s73, 32768\n\t"
    "s_waitcnt lgkmcnt(0)\n\t"
    "s_and_b32 s41, s41, 0xffff\n\t"
    "s_barrier\n\t"
    "ds_read_b128 v[180:183], v248 offset:0\n\t"
    "ds_read_b128 v[184:187], v248 offset:2048\n\t"
    "ds_read_b128 v[188:191], v248 offset:4096\n\t"
    "ds_read_b128 v[192:195], v248 offset:6144\n\t"
    "ds_read_b128 v[144:147], v251 offset:0\n\t"
    "ds_read_b128 v[148:151], v251 offset:2064\n\t"
    "ds_read_b128 v[152:155], v251 offset:4128\n\t"
    "ds_read_b128 v[156:159], v251 offset:6192\n\t"
    "ds_read_b128 v[160:163], v251 offset:8256\n\t"
    "ds_read_b128 v[164:167], v251 offset:10320\n\t"
    "ds_read_b128 v[168:171], v251 offset:12384\n\t"
    "ds_read_b128 v[172:175], v251 offset:14448\n\t"
    "ds_read_b64_tr_b16 v[176:177], v250 offset:0\n\t"
    "s_waitcnt lgkmcnt(0)\n\t"
    "L_cloop%=:\n\t"
    "s_waitcnt lgkmcnt(11)\n\t"
    "v_mfma_f32_16x16x32_bf16 v[0:3], v[180:183], v[144:147], v[0:3]\n\t"
    "ds_read_b64_tr_b16 v[178:179], v250 offset:512\n\t"
    "s_waitcnt lgkmcnt(11)\n\t"
    "v_mfma_f32_16x16x32_bf16 v[36:39], v[184:187], v[144:147], v[36:39]\n\t"
    "ds_read_b128 v[196:199], v249 offset:0\n\t"
    "s_waitcnt lgkmcnt(11)\n\t"
    "v_mfma_f32_16x16x32_bf16 v[72:75], v[188:191], v[144:147], v[72:75]\n\t"
    "ds_read_b128 v[200:203], v249 offset:2048\n\t"
    "s_waitcnt lgkmcnt(11)\n\t"
    "v_mfma_f32_16x16x32_bf16 v[108:111], v[192:195], v[144:147], v[108:111]\n\t"
    "ds_read_b128 v[144:147], v251 offset:1024\n\t"
    "s_waitcnt lgkmcnt(11)\n\t"
    "v_mfma_f32_16x16x32_bf16 v[4:7], v[180:183], v[148:151], v[4:7]\n\t"
    "ds_read_b128 v[204:207], v249 offset:4096\n\t"
    "v_mfma_f32_16x16x32_bf16 v[40:43], v[184:187], v[148:151], v[40:43]\n\t"
    "ds_read_b128 v[208:211], v249 offset:6144\n\t"
    "v_mfma_f32_16x16x32_bf16 v[76:79], v[188:191], v[148:151], v[76:79]\n\t"
    "v_mfma_f32_16x16x32_bf16 v[112:115], v[192:195], v[148:151], v[112:115]\n\t"
    "ds_read_b128 v[148:151], v251 offset:3088\n\t"
    "s_waitcnt lgkmcnt(13)\n\t"
    "v_mfma_f32_16x16x32_bf16 v[8:11], v[180:183], v[152:155], v[8:11]\n\t"
    "v_mfma_f32_16x16x32_bf16 v[44:47], v[184:187], v[152:155], v[44:47]\n\t"
    "v_mfma_f32_16x16x32_bf16 v[80:83], v[188:191], v[152:155], v[80:83]\n\t"
    "v_mfma_f32_16x16x32_bf16 v[116:119], v[192:195], v[152:155], v[116:119]\n\t"
    "ds_read_b128 v[152:155], v251 offset:5152\n\t"
    "s_waitcnt lgkmcnt(13)\n\t"
    "v_mfma_f32_16x16x32_bf16 v[12:15], v[180:183], v[156:159], v[12:15]\n\t"
    "v_mfma_f32_16x16x32_bf16 v[48:51], v[184:187], v[156:159], v[48:51]\n\t"
    "v_mfma_f32_16x16x32_bf16 v[84:87], v[188:191], v[156:159], v[84:87]\n\t"
    "v_mfma_f32_16x16x32_bf16 v[120:123], v[192:195], v[156:159], v[120:123]\n\t"
    "ds_read_b128 v[156:159], v251 offset:7216\n\t"
    "s_waitcnt lgkmcnt(13)\n\t"
    "v_mfma_f32_16x16x32_bf16 v[16:19], v[180:183], v[160:163], v[16:19]\n\t"
    "v_mfma_f32_16x16x32_bf16 v[52:55], v[184:187], v[160:163], v[52:55]\n\t"
    "v_mfma_f32_16x16x32_bf16 v[88:91], v[188:191], v[160:163], v[88:91]\n\t"
    "v_mfma_f32_16x16x32_bf16 v[124:127], v[192:195], v[160:163], v[124:127]\n\t"
    "ds_read_b128 v[160:163], v251 offset:9280\n\t"
    "s_waitcnt lgkmcnt(13)\n\t"
    "v_mfma_f32_16x16x32_bf16 v[20:23], v[180:183], v[164:167], v[20:23]\n\t"
    "v_mfma_f32_16x16x32_bf16 v[56:59], v[184:187], v[164:167], v[56:59]\n\t"
    "v_mfma_f32_16x16x32_bf16 v[92:95], v[188:191], v[164:167], v[92:95]\n\t"
    "v_mfma_f32_16x16x32_bf16 v[128:131], v[192:195], v[164:167], v[128:131]\n\t"
    "ds_read_b128 v[164:167], v251 offset:11344\n\t"
    "s_waitcnt lgkmcnt(13)\n\t"
    "v_mfma_f32_16x16x32_bf16 v[24:27], v[180:183], v[168:171], v[24:27]\n\t"
    "v_mfma_f32_16x16x32_bf16 v[60:63], v[184:187], v[168:171], v[60:63]\n\t"
    "v_mfma_f32_16x16x32_bf16 v[96:99], v[188:191], v[168:171], v[96:99]\n\t"
    "v_mfma_f32_16x16x32_bf16 v[132:135], v[192:195], v[168:171], v[132:135]\n\t"
    "ds_read_b128 v[168:171], v251 offset:13408\n\t"
    "s_waitcnt lgkmcnt(13)\n\t"
    "v_mfma_f32_16x16x32_bf16 v[28:31], v[180:183], v[172:175], v[28:31]\n\t"
    "v_mfma_f32_16x16x32_bf16 v[64:67], v[184:187], v[172:175], v[64:67]\n\t"
    "v_mfma_f32_16x16x32_bf16 v[100:103], v[188:191], v[172:175], v[100:103]\n\t"
    "v_mfma_f32_16x16x32_bf16 v[136:139], v[192:195], v[172:175], v[136:139]\n\t"
    "ds_read_b128 v[172:175], v251 offset:15472\n\t"
    "s_waitcnt lgkmcnt(12)\n\t"
    "v_mfma_f32_16x16x32_bf16 v[32:35], v[180:183], v[176:179], v[32:35]\n\t"
    "v_mfma_f32_16x16x32_bf16 v[68:71], v[184:187], v[176:179], v[68:71]\n\t"
    "v_mfma_f32_16x16x32_bf16 v[104:107], v[188:191], v[176:179], v[104:107]\n\t"
    "v_mfma_f32_16x16x32_bf16 v[140:143], v[192:195], v[176:179], v[140:143]\n\t"
    "ds_read_b64_tr_b16 v[176:177], v250 offset:1024\n\t"
    "s_waitcnt lgkmcnt(10)\n\t"
    "v_mfma_f32_16x16x32_bf16 v[0:3], v[196:199], v[144:147], v[0:3]\n\t"
    "ds_read_b64_tr_b16 v[178:179], v250 offset:1536\n\t"
    "s_add_u32 s80, s80, 1\n\t"
    "v_mfma_f32_16x16x32_bf16 v[36:39], v[200:203], v[144:147], v[36:39]\n\t"
    "s_cmp_eq_u32 s80, 3\n\t"
    "s_waitcnt lgkmcnt(10)\n\t"
    "v_mfma_f32_16x16x32_bf16 v[72:75], v[204:207], v[144:147], v[72:75]\n\t"
    "s_cselect_b32 s70, s72, s73\n\t"
    "s_waitcnt lgkmcnt(9)\n\t"
    "v_mfma_f32_16x16x32_bf16 v[108:111], v[208:211], v[144:147], v[108:111]\n\t"
    "s_cselect_b32 s80, 0, s80\n\t"
    "s_waitcnt lgkmcnt(8)\n\t"
    "v_mfma_f32_16x16x32_bf16 v[4:7], v[196:199], v[148:151], v[4:7]\n\t"
    "v_add_u32 v248, s70, v248\n\t"
    "v_mfma_f32_16x16x32_bf16 v[40:43], v[200:203], v[148:151], v[40:43]\n\t"
    "v_add_u32 v249, s70, v249\n\t"
    "v_mfma_f32_16x16x32_bf16 v[76:79], v[204:207], v[148:151], v[76:79]\n\t"
    "v_add_u32 v250, s81, v250\n\t"
    "v_mfma_f32_16x16x32_bf16 v[112:115], v[208:211], v[148:151], v[112:115]\n\t"
    "v_add_u32 v251, s81, v251\n\t"
    "s_waitcnt lgkmcnt(7)\n\t"
    "v_mfma_f32_16x16x32_bf16 v[8:11], v[196:199], v[152:155], v[8:11]\n\t"
    "s_sub_u32 s81, 0, s81\n\t"
    "v_mfma_f32_16x16x32_bf16 v[44:47], v[200:203], v[152:155], v[44:47]\n\t"
    "s_add_u32 s79, s79, 1\n\t"
    "v_mfma_f32_16x16x32_bf16 v[80:83], v[204:207], v[152:155], v[80:83]\n\t"
    "v_mfma_f32_16x16x32_bf16 v[116:119], v[208:211], v[152:155], v[116:119]\n\t"
    "s_waitcnt lgkmcnt(6)\n\t"
    "v_mfma_f32_16x16x32_bf16 v[12:15], v[196:199], v[156:159], v[12:15]\n\t"
    "v_mfma_f32_16x16x32_bf16 v[48:51], v[200:203], v[156:159], v[48:51]\n\t"
    "v_mfma_f32_16x16x32_bf16 v[84:87], v[204:207], v[156:159], v[84:87]\n\t"
    "v_mfma_f32_16x16x32_bf16 v[120:123], v[208:211], v[156:159], v[120:123]\n\t"
    "s_waitcnt lgkmcnt(5)\n\t"
    "v_mfma_f32_16x16x32_bf16 v[16:19], v[196:199], v[160:163], v[16:19]\n\t"
    "v_mfma_f32_16x16x32_bf16 v[52:55], v[200:203], v[160:163], v[52:55]\n\t"
    "v_mfma_f32_16x16x32_bf16 v[88:91], v[204:207], v[160:163], v[88:91]\n\t"
    "v_mfma_f32_16x16x32_bf16 v[124:127], v[208:211], v[160:163], v[124:127]\n\t"
    "s_waitcnt lgkmcnt(4)\n\t"
    "v_mfma_f32_16x16x32_bf16 v[20:23], v[196:199], v[164:167], v[20:23]\n\t"
    "v_mfma_f32_16x16x32_bf16 v[56:59], v[200:203], v[164:167], v[56:59]\n\t"
    "v_mfma_f32_16x16x32_bf16 v[92:95], v[204:207], v[164:167], v[92:95]\n\t"
    "v_mfma_f32_16x16x32_bf16 v[128:131], v[208:211], v[164:167], v[128:131]\n\t"
    "s_waitcnt lgkmcnt(0)\n\t"
    "s_barrier\n\t"
    "ds_read_b128 v[144:147], v251 offset:0\n\t"
    "ds_read_b128 v[180:183], v248 offset:0\n\t"
    "v_mfma_f32_16x16x32_bf16 v[24:27], v[196:199], v[168:171], v[24:27]\n\t"
    "ds_read_b128 v[184:187], v248 offset:2048\n\t"
    "ds_read_b128 v[188:191], v248 offset:4096\n\t"
    "v_mfma_f32_16x16x32_bf16 v[60:63], v[200:203], v[168:171], v[60:63]\n\t"
    "ds_read_b128 v[192:195], v248 offset:6144\n\t"
    "ds_read_b128 v[148:151], v251 offset:2064\n\t"
    "v_mfma_f32_16x16x32_bf16 v[96:99], v[204:207], v[168:171], v[96:99]\n\t"
    "ds_read_b128 v[152:155], v251 offset:4128\n\t"
    "ds_read_b128 v[156:159], v251 offset:6192\n\t"
    "v_mfma_f32_16x16x32_bf16 v[132:135], v[208:211], v[168:171], v[132:135]\n\t"
    "ds_read_b128 v[160:163], v251 offset:8256\n\t"
    "ds_read_b128 v[164:167], v251 offset:10320\n\t"
    "v_mfma_f32_16x16x32_bf16 v[28:31], v[196:199], v[172:175], v[28:31]\n\t"
    "ds_read_b128 v[168:171], v251 offset:12384\n\t"
    "v_mfma_f32_16x16x32_bf16 v[64:67], v[200:203], v[172:175], v[64:67]\n\t"
    "v_mfma_f32_16x16x32_bf16 v[100:103], v[204:207], v[172:175], v[100:103]\n\t"
    "v_mfma_f32_16x16x32_bf16 v[136:139], v[208:211], v[172:175], v[136:139]\n\t"
    "ds_read_b128 v[172:175], v251 offset:14448\n\t"
    "v_mfma_f32_16x16x32_bf16 v[32:35], v[196:199], v[176:179], v[32:35]\n\t"
    "v_mfma_f32_16x16x32_bf16 v[68:71], v[200:203], v[176:179], v[68:71]\n\t"
    "v_mfma_f32_16x16x32_bf16 v[104:107], v[204:207], v[176:179], v[104:107]\n\t"
    "v_mfma_f32_16x16x32_bf16 v[140:143], v[208:211], v[176:179], v[140:143]\n\t"
    "ds_read_b64_tr_b16 v[176:177], v250 offset:0\n\t"
    "s_cmp_lt_u32 s79, 8\n\t"
    "s_cbranch_scc1 L_cloop%=\n\t"
    "s_waitcnt lgkmcnt(11)\n\t"
    "v_mfma_f32_16x16x32_bf16 v[0:3], v[180:183], v[144:147], v[0:3]\n\t"
    "ds_read_b64_tr_b16 v[178:179], v250 offset:512\n\t"
    "ds_read_b128 v[196:199], v249 offset:0\n\t"
    "s_waitcnt lgkmcnt(9)\n\t"
    "v_mfma_f32_16x16x32_bf16 v[4:7], v[180:183], v[148:151], v[4:7]\n\t"
    "ds_read_b128 v[212:215], v251 offset:1024\n\t"
    "ds_read_b128 v[216:219], v251 offset:3088\n\t"
    "s_waitcnt lgkmcnt(10)\n\t"
    "v_mfma_f32_16x16x32_bf16 v[8:11], v[180:183], v[152:155], v[8:11]\n\t"
    "ds_read_b128 v[220:223], v251 offset:5152\n\t"
    "ds_read_b128 v[224:227], v251 offset:7216\n\t"
    "s_waitcnt lgkmcnt(11)\n\t"
    "v_mfma_f32_16x16x32_bf16 v[12:15], v[180:183], v[156:159], v[12:15]\n\t"
    "ds_read_b128 v[228:231], v251 offset:9280\n\t"
    "ds_read_b128 v[232:235], v251 offset:11344\n\t"
    "s_waitcnt lgkmcnt(12)\n\t"
    "v_mfma_f32_16x16x32_bf16 v[16:19], v[180:183], v[160:163], v[16:19]\n\t"
    "ds_read_b128 v[236:239], v251 offset:13408\n\t"
    "ds_read_b128 v[240:243], v251 offset:15472\n\t"
    "s_waitcnt lgkmcnt(13)\n\t"
    "v_mfma_f32_16x16x32_bf16 v[20:23], v[180:183], v[164:167], v[20:23]\n\t"
    "ds_read_b64_tr_b16 v[244:245], v250 offset:1024\n\t"
    "ds_read_b64_tr_b16 v[246:247], v250 offset:1536\n\t"
    "s_waitcnt lgkmcnt(14)\n\t"
    "v_mfma_f32_16x16x32_bf16 v[24:27], v[180:183], v[168:171], v[24:27]\n\t"
    "ds_read_b128 v[200:203], v249 offset:2048\n\t"
    "ds_read_b128 v[204:207], v249 offset:4096\n\t"
    "s_waitcnt lgkmcnt(15)\n\t"
    "v_mfma_f32_16x16x32_bf16 v[28:31], v[180:183], v[172:175], v[28:31]\n\t"
    "ds_read_b128 v[208:211], v249 offset:6144\n\t"
    "s_waitcnt lgkmcnt(14)\n\t"
    "v_mfma_f32_16x16x32_bf16 v[32:35], v[180:183], v[176:179], v[32:35]\n\t"
    "v_mfma_f32_16x16x32_bf16 v[36:39], v[184:187], v[144:147], v[36:39]\n\t"
    "v_mfma_f32_16x16x32_bf16 v[40:43], v[184:187], v[148:151], v[40:43]\n\t"
    "v_mfma_f32_16x16x32_bf16 v[44:47], v[184:187], v[152:155], v[44:47]\n\t"
    "v_mfma_f32_16x16x32_bf16 v[48:51], v[184:187], v[156:159], v[48:51]\n\t"
    "v_mfma_f32_16x16x32_bf16 v[52:55], v[184:187], v[160:163], v[52:55]\n\t"
    "v_mfma_f32_16x16x32_bf16 v[56:59], v[184:187], v[164:167], v[56:59]\n\t"
    "v_mfma_f32_16x16x32_bf16 v[60:63], v[184:187], v[168:171], v[60:63]\n\t"
    "v_mfma_f32_16x16x32_bf16 v[64:67], v[184:187], v[172:175], v[64:67]\n\t"
    "v_mfma_f32_16x16x32_bf16 v[68:71], v[184:187], v[176:179], v[68:71]\n\t"
    "s_waitcnt lgkmcnt(12)\n\t"
    "v_mfma_f32_16x16x32_bf16 v[0:3], v[196:199], v[212:215], v[0:3]\n\t"
    "s_waitcnt lgkmcnt(11)\n\t"
    "v_mfma_f32_16x16x32_bf16 v[4:7], v[196:199], v[216:219], v[4:7]\n\t"
    "s_waitcnt lgkmcnt(10)\n\t"
    "v_mfma_f32_16x16x32_bf16 v[8:11], v[196:199], v[220:223], v[8:11]\n\t"
    "s_waitcnt lgkmcnt(9)\n\t"
    "v_mfma_f32_16x16x32_bf16 v[12:15], v[196:199], v[224:227], v[12:15]\n\t"
    "s_waitcnt lgkmcnt(8)\n\t"
    "v_mfma_f32_16x16x32_bf16 v[16:19], v[196:199], v[228:231], v[16:19]\n\t"
    "s_waitcnt lgkmcnt(7)\n\t"
    "v_mfma_f32_16x16x32_bf16 v[20:23], v[196:199], v[232:235], v[20:23]\n\t"
    "s_waitcnt lgkmcnt(6)\n\t"
    "v_mfma_f32_16x16x32_bf16 v[24:27], v[196:199], v[236:239], v[24:27]\n\t"
    "s_waitcnt lgkmcnt(5)\n\t"
    "v_mfma_f32_16x16x32_bf16 v[28:31], v[196:199], v[240:243], v[28:31]\n\t"
    "s_waitcnt lgkmcnt(3)\n\t"
    "v_mfma_f32_16x16x32_bf16 v[32:35], v[196:199], v[244:247], v[32:35]\n\t"
    "s_waitcnt lgkmcnt(2)\n\t"
    "v_mfma_f32_16x16x32_bf16 v[36:39], v[200:203], v[212:215], v[36:39]\n\t"
    "v_mfma_f32_16x16x32_bf16 v[40:43], v[200:203], v[216:219], v[40:43]\n\t"
    "v_cvt_pk_bf16_f32 v180, v0, v4\n\t"
    "v_cvt_pk_bf16_f32 v181, v8, v12\n\t"
    "v_mfma_f32_16x16x32_bf16 v[44:47], v[200:203], v[220:223], v[44:47]\n\t"
    "v_cvt_pk_bf16_f32 v182, v16, v20\n\t"
    "v_cvt_pk_bf16_f32 v183, v24, v28\n\t"
    "v_mfma_f32_16x16x32_bf16 v[48:51], v[200:203], v[224:227], v[48:51]\n\t"
    "buffer_store_dwordx4 v[180:183], v252, s[40:43], s44 offen nt\n\t"
    "v_cvt_pk_bf16_f32 v196, v1, v5\n\t"
    "v_mfma_f32_16x16x32_bf16 v[52:55], v[200:203], v[228:231], v[52:55]\n\t"
    "v_cvt_pk_bf16_f32 v197, v9, v13\n\t"
    "v_cvt_pk_bf16_f32 v198, v17, v21\n\t"
    "v_mfma_f32_16x16x32_bf16 v[56:59], v[200:203], v[232:235], v[56:59]\n\t"
    "v_cvt_pk_bf16_f32 v199, v25, v29\n\t"
    "buffer_store_dwordx4 v[196:199], v252, s[40:43], s45 offen nt\n\t"
    "v_mfma_f32_16x16x32_bf16 v[60:63], v[200:203], v[236:239], v[60:63]\n\t"
    "v_cvt_pk_bf16_f32 v180, v2, v6\n\t"
    "v_cvt_pk_bf16_f32 v181, v10, v14\n\t"
    "v_mfma_f32_16x16x32_bf16 v[64:67], v[200:203], v[240:243], v[64:67]\n\t"
    "v_cvt_pk_bf16_f32 v182, v18, v22\n\t"
    "v_cvt_pk_bf16_f32 v183, v26, v30\n\t"
    "v_mfma_f32_16x16x32_bf16 v[68:71], v[200:203], v[244:247], v[68:71]\n\t"
    "buffer_store_dwordx4 v[180:183], v252, s[40:43], s46 offen nt\n\t"
    "v_cvt_pk_bf16_f32 v196, v3, v7\n\t"
    "v_mfma_f32_16x16x32_bf16 v[72:75], v[188:191], v[144:147], v[72:75]\n\t"
    "v_cvt_pk_bf16_f32 v197, v11, v15\n\t"
    "v_cvt_pk_bf16_f32 v198, v19, v23\n\t"
    "v_mfma_f32_16x16x32_bf16 v[76:79], v[188:191], v[148:151], v[76:79]\n\t"
    "v_cvt_pk_bf16_f32 v199, v27, v31\n\t"
    "buffer_store_dwordx4 v[196:199], v252, s[40:43], s47 offen nt\n\t"
    "v_mfma_f32_16x16x32_bf16 v[80:83], v[188:191], v[152:155], v[80:83]\n\t"
    "v_cvt_pk_bf16_f32 v180, v32, v33\n\t"
    "v_cvt_pk_bf16_f32 v181, v34, v35\n\t"
    "v_mfma_f32_16x16x32_bf16 v[84:87], v[188:191], v[156:159], v[84:87]\n\t"
    "buffer_store_short v180, v253, s[40:43], s44 offen\n\t"
    "buffer_store_short_d16_hi v180, v253, s[40:43], s45 offen\n\t"
    "v_mfma_f32_16x16x32_bf16 v[88:91], v[188:191], v[160:163], v[88:91]\n\t"
    "buffer_store_short v181, v253, s[40:43], s46 offen\n\t"
    "buffer_store_short_d16_hi v181, v253, s[40:43], s47 offen\n\t"
    "v_mfma_f32_16x16x32_bf16 v[92:95], v[188:191], v[164:167], v[92:95]\n\t"
    "v_cvt_pk_bf16_f32 v184, v36, v40\n\t"
    "v_cvt_pk_bf16_f32 v185, v44, v48\n\t"
    "v_mfma_f32_16x16x32_bf16 v[96:99], v[188:191], v[168:171], v[96:99]\n\t"
    "v_cvt_pk_bf16_f32 v186, v52, v56\n\t"
    "v_cvt_pk_bf16_f32 v187, v60, v64\n\t"
    "v_mfma_f32_16x16x32_bf16 v[100:103], v[188:191], v[172:175], v[100:103]\n\t"
    "buffer_store_dwordx4 v[184:187], v252, s[40:43], s48 offen nt\n\t"
    "v_cvt_pk_bf16_f32 v200, v37, v41\n\t"
    "v_mfma_f32_16x16x32_bf16 v[104:107], v[188:191], v[176:179], v[104:107]\n\t"
    "v_cvt_pk_bf16_f32 v201, v45, v49\n\t"
    "v_cvt_pk_bf16_f32 v202, v53, v57\n\t"
    "s_waitcnt lgkmcnt(1)\n\t"
    "v_mfma_f32_16x16x32_bf16 v[72:75], v[204:207], v[212:215], v[72:75]\n\t"
    "v_cvt_pk_bf16_f32 v203, v61, v65\n\t"
    "buffer_store_dwordx4 v[200:203], v252, s[40:43], s49 offen nt\n\t"
    "v_mfma_f32_16x16x32_bf16 v[76:79], v[204:207], v[216:219], v[76:79]\n\t"
    "v_cvt_pk_bf16_f32 v184, v38, v42\n\t"
    "v_cvt_pk_bf16_f32 v185, v46, v50\n\t"
    "v_mfma_f32_16x16x32_bf16 v[80:83], v[204:207], v[220:223], v[80:83]\n\t"
    "v_cvt_pk_bf16_f32 v186, v54, v58\n\t"
    "v_cvt_pk_bf16_f32 v187, v62, v66\n\t"
    "v_mfma_f32_16x16x32_bf16 v[84:87], v[204:207], v[224:227], v[84:87]\n\t"
    "buffer_store_dwordx4 v[184:187], v252, s[40:43], s50 offen nt\n\t"
    "v_cvt_pk_bf16_f32 v200, v39, v43\n\t"
    "v_mfma_f32_16x16x32_bf16 v[88:91], v[204:207], v[228:231], v[88:91]\n\t"
    "v_cvt_pk_bf16_f32 v201, v47, v51\n\t"
    "v_cvt_pk_bf16_f32 v202, v55, v59\n\t"
    "v_mfma_f32_16x16x32_bf16 v[92:95], v[204:207], v[232:235], v[92:95]\n\t"
    "v_cvt_pk_bf16_f32 v203, v63, v67\n\t"
    "buffer_store_dwordx4 v[200:203], v252, s[40:43], s51 offen nt\n\t"
    "v_mfma_f32_16x16x32_bf16 v[96:99], v[204:207], v[236:239], v[96:99]\n\t"
    "v_cvt_pk_bf16_f32 v184, v68, v69\n\t"
    "v_cvt_pk_bf16_f32 v185, v70, v71\n\t"
    "v_mfma_f32_16x16x32_bf16 v[100:103], v[204:207], v[240:243], v[100:103]\n\t"
    "buffer_store_short v184, v253, s[40:43], s48 offen\n\t"
    "buffer_store_short_d16_hi v184, v253, s[40:43], s49 offen\n\t"
    "v_mfma_f32_16x16x32_bf16 v[104:107], v[204:207], v[244:247], v[104:107]\n\t"
    "buffer_store_short v185, v253, s[40:43], s50 offen\n\t"
    "buffer_store_short_d16_hi v185, v253, s[40:43], s51 offen\n\t"
    "v_mfma_f32_16x16x32_bf16 v[108:111], v[192:195], v[144:147], v[108:111]\n\t"
    "v_mfma_f32_16x16x32_bf16 v[112:115], v[192:195], v[148:151], v[112:115]\n\t"
    "v_cvt_pk_bf16_f32 v188, v72, v76\n\t"
    "v_cvt_pk_bf16_f32 v189, v80, v84\n\t"
    "v_mfma_f32_16x16x32_bf16 v[116:119], v[192:195], v[152:155], v[116:119]\n\t"
    "v_cvt_pk_bf16_f32 v190, v88, v92\n\t"
    "v_cvt_pk_bf16_f32 v191, v96, v100\n\t"
    "v_mfma_f32_16x16x32_bf16 v[120:123], v[192:195], v[156:159], v[120:123]\n\t"
    "buffer_store_dwordx4 v[188:191], v252, s[40:43], s52 offen nt\n\t"
    "v_cvt_pk_bf16_f32 v204, v73, v77\n\t"
    "v_mfma_f32_16x16x32_bf16 v[124:127], v[192:195], v[160:163], v[124:127]\n\t"
    "v_cvt_pk_bf16_f32 v205, v81, v85\n\t"
    "v_cvt_pk_bf16_f32 v206, v89, v93\n\t"
    "v_mfma_f32_16x16x32_bf16 v[128:131], v[192:195], v[164:167], v[128:131]\n\t"
    "v_cvt_pk_bf16_f32 v207, v97, v101\n\t"
    "buffer_store_dwordx4 v[204:207], v252, s[40:43], s53 offen nt\n\t"
    "v_mfma_f32_16x16x32_bf16 v[132:135], v[192:195], v[168:171], v[132:135]\n\t"
    "v_cvt_pk_bf16_f32 v188, v74, v78\n\t"
    "v_cvt_pk_bf16_f32 v189, v82, v86\n\t"
    "v_mfma_f32_16x16x32_bf16 v[136:139], v[192:195], v[172:175], v[136:139]\n\t"
    "v_cvt_pk_bf16_f32 v190, v90, v94\n\t"
    "v_cvt_pk_bf16_f32 v191, v98, v102\n\t"
    "v_mfma_f32_16x16x32_bf16 v[140:143], v[192:195], v[176:179], v[140:143]\n\t"
    "buffer_store_dwordx4 v[188:191], v252, s[40:43], s54 offen nt\n\t"
    "v_cvt_pk_bf16_f32 v204, v75, v79\n\t"
    "s_waitcnt lgkmcnt(0)\n\t"
    "v_mfma_f32_16x16x32_bf16 v[108:111], v[208:211], v[212:215], v[108:111]\n\t"
    "v_cvt_pk_bf16_f32 v205, v83, v87\n\t"
    "v_cvt_pk_bf16_f32 v206, v91, v95\n\t"
    "v_mfma_f32_16x16x32_bf16 v[112:115], v[208:211], v[216:219], v[112:115]\n\t"
    "v_cvt_pk_bf16_f32 v207, v99, v103\n\t"
    "buffer_store_dwordx4 v[204:207], v252, s[40:43], s55 offen nt\n\t"
    "v_mfma_f32_16x16x32_bf16 v[116:119], v[208:211], v[220:223], v[116:119]\n\t"
    "v_cvt_pk_bf16_f32 v188, v104, v105\n\t"
    "v_cvt_pk_bf16_f32 v189, v106, v107\n\t"
    "v_mfma_f32_16x16x32_bf16 v[120:123], v[208:211], v[224:227], v[120:123]\n\t"
    "buffer_store_short v188, v253, s[40:43], s52 offen\n\t"
    "buffer_store_short_d16_hi v188, v253, s[40:43], s53 offen\n\t"
    "v_mfma_f32_16x16x32_bf16 v[124:127], v[208:211], v[228:231], v[124:127]\n\t"
    "buffer_store_short v189, v253, s[40:43], s54 offen\n\t"
    "buffer_store_short_d16_hi v189, v253, s[40:43], s55 offen\n\t"
    "v_mfma_f32_16x16x32_bf16 v[128:131], v[208:211], v[232:235], v[128:131]\n\t"
    "v_mfma_f32_16x16x32_bf16 v[132:135], v[208:211], v[236:239], v[132:135]\n\t"
    "v_mfma_f32_16x16x32_bf16 v[136:139], v[208:211], v[240:243], v[136:139]\n\t"
    "v_mfma_f32_16x16x32_bf16 v[140:143], v[208:211], v[244:247], v[140:143]\n\t"
    "v_cvt_pk_bf16_f32 v192, v108, v112\n\t"
    "v_cvt_pk_bf16_f32 v193, v116, v120\n\t"
    "s_nop 2\n\t"
    "v_cvt_pk_bf16_f32 v194, v124, v128\n\t"
    "s_nop 2\n\t"
    "v_cvt_pk_bf16_f32 v195, v132, v136\n\t"
    "buffer_store_dwordx4 v[192:195], v252, s[40:43], s56 offen nt\n\t"
    "v_cvt_pk_bf16_f32 v208, v109, v113\n\t"
    "v_cvt_pk_bf16_f32 v209, v117, v121\n\t"
    "v_cvt_pk_bf16_f32 v210, v125, v129\n\t"
    "v_cvt_pk_bf16_f32 v211, v133, v137\n\t"
    "buffer_store_dwordx4 v[208:211], v252, s[40:43], s57 offen nt\n\t"
    "v_cvt_pk_bf16_f32 v192, v110, v114\n\t"
    "v_cvt_pk_bf16_f32 v193, v118, v122\n\t"
    "v_cvt_pk_bf16_f32 v194, v126, v130\n\t"
    "v_cvt_pk_bf16_f32 v195, v134, v138\n\t"
    "buffer_store_dwordx4 v[192:195], v252, s[40:43], s58 offen nt\n\t"
    "v_cvt_pk_bf16_f32 v208, v111, v115\n\t"
    "v_cvt_pk_bf16_f32 v209, v119, v123\n\t"
    "v_cvt_pk_bf16_f32 v210, v127, v131\n\t"
    "v_cvt_pk_bf16_f32 v211, v135, v139\n\t"
    "buffer_store_dwordx4 v[208:211], v252, s[40:43], s59 offen nt\n\t"
    "v_cvt_pk_bf16_f32 v192, v140, v141\n\t"
    "v_cvt_pk_bf16_f32 v193, v142, v143\n\t"
    "buffer_store_short v192, v253, s[40:43], s56 offen\n\t"
    "buffer_store_short_d16_hi v192, v253, s[40:43], s57 offen\n\t"
    "buffer_store_short v193, v253, s[40:43], s58 offen\n\t"
    "buffer_store_short_d16_hi v193, v253, s[40:43], s59 offen\n\t"
    "s_endpgm\n\t"
    "L_end%=:\n\t"
    :
    : [bid] "s"(bid), [wv] "s"(wv), [karg] "s"(karg), [ldsb] "s"(ldsb)
    : "memory", "vcc", "scc", "s4", "s5", "s6", "s7", "s8", "s9", "s10", "s11", "s19", "s20", "s21", "s22", "s23", "s24", "s25", "s26", "s27", "s28", "s29", "s30", "s31", "s36", "s37", "s38", "s39", "s40", "s41", "s42", "s43", "s44", "s45", "s46", "s47", "s48", "s49", "s50", "s51", "s52", "s53", "s54", "s55", "s56", "s57", "s58", "s59", "s60", "s61", "s62", "s63", "s64", "s65", "s66", "s67", "s68", "s69", "s70", "s71", "s72", "s73", "s74", "s75", "s76", "s77", "s78", "s79", "s80", "s81", "s82", "s83", "s84", "s85", "s86", "s87", "s88", "s89", "s90", "s91", "s92", "s93", "s94", "s95", "s96", "s97",
      "v0", "v1", "v2", "v3", "v4", "v5", "v6", "v7", "v8", "v9", "v10", "v11", "v12", "v13", "v14", "v15", "v16", "v17", "v18", "v19", "v20", "v21", "v22", "v23", "v24", "v25", "v26", "v27", "v28", "v29", "v30", "v31", "v32", "v33", "v34", "v35", "v36", "v37", "v38", "v39", "v40", "v41", "v42", "v43", "v44", "v45", "v46", "v47", "v48", "v49", "v50", "v51", "v52", "v53", "v54", "v55", "v56", "v57", "v58", "v59", "v60", "v61", "v62", "v63", "v64", "v65", "v66", "v67", "v68", "v69", "v70", "v71", "v72", "v73", "v74", "v75", "v76", "v77", "v78", "v79", "v80", "v81", "v82", "v83", "v84", "v85", "v86", "v87", "v88", "v89", "v90", "v91", "v92", "v93", "v94", "v95", "v96", "v97", "v98", "v99", "v100", "v101", "v102", "v103", "v104", "v105", "v106", "v107", "v108", "v109", "v110", "v111", "v112", "v113", "v114", "v115", "v116", "v117", "v118", "v119", "v120", "v121", "v122", "v123", "v124", "v125", "v126", "v127", "v128", "v129", "v130", "v131", "v132", "v133", "v134", "v135", "v136", "v137", "v138", "v139", "v140", "v141", "v142", "v143", "v144", "v145", "v146", "v147", "v148", "v149", "v150", "v151", "v152", "v153", "v154", "v155", "v156", "v157", "v158", "v159", "v160", "v161", "v162", "v163", "v164", "v165", "v166", "v167", "v168", "v169", "v170", "v171", "v172", "v173", "v174", "v175", "v176", "v177", "v178", "v179", "v180", "v181", "v182", "v183", "v184", "v185", "v186", "v187", "v188", "v189", "v190", "v191", "v192", "v193", "v194", "v195", "v196", "v197", "v198", "v199", "v200", "v201", "v202", "v203", "v204", "v205", "v206", "v207", "v208", "v209", "v210", "v211", "v212", "v213", "v214", "v215", "v216", "v217", "v218", "v219", "v220", "v221", "v222", "v223", "v224", "v225", "v226", "v227", "v228", "v229", "v230", "v231", "v232", "v233", "v234", "v235", "v236", "v237", "v238", "v239", "v240", "v241", "v242", "v243", "v244", "v245", "v246", "v247", "v248", "v249", "v250", "v251", "v252", "v253", "v254", "v255");
}

extern "C" void kernel_launch(void* const* d_in, const int* in_sizes, int n_in, void* d_out, int out_size,
                              void* d_ws, size_t ws_size, hipStream_t stream) {
  standalone__weight_only_int_omniquant__prompt_kernel<<<256, 512, 0, stream>>>((const char*)d_in[0], (const char*)d_in[1], (const char*)d_in[2],
      (const char*)d_in[3], (const char*)d_in[4], (char*)d_out);
}
```
